```python
import torch

def _softmax(x, axis=-1):
    return torch.softmax(x, dim=axis)
def _gelu(x):
    return torch.nn.functional.gelu(x, approximate="tanh")
def _silu(x):
    return torch.nn.functional.silu(x)
def _sigmoid(x):
    return torch.sigmoid(x)
def _layernorm(x, axis=-1, eps=1e-5):
    m = x.mean(dim=axis, keepdim=True)
    v = x.var(dim=axis, keepdim=True, unbiased=False)
    return (x - m) / torch.sqrt(v + eps)
def _scatter_add(tgt, idx, src, axis=0):
    return tgt.clone().index_add_(axis, idx, src)
def _segment_sum(src, seg, n):
    out = torch.zeros((n,) + src.shape[1:], dtype=src.dtype)
    return out.index_add_(0, seg, src)
def _pad(x, pw):
    flat = tuple(v for pair in reversed(pw) for v in pair)
    return torch.nn.functional.pad(x, flat)

kernel_name = 'standalone__paged_block_table_kv_pagedattention__decode'

def setup_inputs():

    _w = (lambda a: (lambda t: t.to(torch.bfloat16) if t.is_floating_point() else t)(torch.as_tensor(a)))
    B, Hq, Hkv, Sq, Skv, D = 1, 16, 8, 1, 65536, 256
    bs = 16
    nbps = max(1, Skv // bs)
    num_phys = B * nbps + 4
    bt = torch.randperm(num_phys)[: B * nbps].reshape(B, nbps).to(torch.int64)
    cl = (nbps * bs - (torch.arange(1, B + 1, dtype=torch.int64) * 2654435761) % max(1, nbps * bs // 2)).to(torch.int64)
    return {
        "q": _w((lambda *s: torch.randn(*s, dtype=torch.bfloat16) * 0.8)(B, Hq, Sq, D)),
        "k_cache": _w((lambda *s: torch.randn(*s, dtype=torch.bfloat16) * 0.8)(num_phys, bs, Hkv, D)),
        "v_cache": _w((lambda *s: torch.randn(*s, dtype=torch.bfloat16) * 0.05)(num_phys, bs, Hkv, D)),
        "block_table": _w(bt),
        "context_lens": _w(cl),
    }

def reference(q, k_cache, v_cache, block_table, context_lens):
    B, Hq, Sq, D = q.shape
    bs = k_cache.shape[1]
    Hkv = k_cache.shape[2]
    nbps = block_table.shape[1]
    Skv = nbps * bs
    bt = block_table.reshape(-1)
    k = (lambda x, *d: x.permute(*d))(k_cache[bt].reshape(B, Skv, Hkv, D), 0, 2, 1, 3)
    v = (lambda x, *d: x.permute(*d))(v_cache[bt].reshape(B, Skv, Hkv, D), 0, 2, 1, 3)
    g = Hq // Hkv
    qg = q.reshape(B, Hkv, g, Sq, D)
    scores = torch.einsum("bhgqd,bhkd->bhgqk", qg, k) * (D ** -0.5)
    pos = torch.arange(Skv)
    msk = pos[None, :] < context_lens[:, None]
    scores = torch.where(msk[:, None, None, None, :], scores, -1e9)
    p = _softmax(scores, axis=-1)
    out = torch.einsum("bhgqk,bhkd->bhgqd", p, v)
    return out.reshape(B, Hq, Sq, D)
```

```cpp
#include <hip/hip_runtime.h>
#include <hip/hip_bf16.h>
#include <stdint.h>

namespace {

constexpr int D = 256, PAGE = 16, HKV = 8, G = 2;
#define NW 8
#define NSPLIT 32
#define KV_AUX 2
constexpr int T = NW * NSPLIT;
constexpr unsigned PAGE_BYTES = PAGE * HKV * D * 2;
constexpr unsigned TOK_BYTES = HKV * D * 2;
constexpr float NEG_BIG = -3.0e38f;

typedef unsigned u32x4 __attribute__((ext_vector_type(4)));
typedef float f32x4 __attribute__((ext_vector_type(4)));
typedef float f32x2 __attribute__((ext_vector_type(2)));
typedef __bf16 bf16x2 __attribute__((ext_vector_type(2)));

__device__ __forceinline__ float dot2(unsigned a, unsigned b, float c) {
  return __builtin_amdgcn_fdot2_f32_bf16(__builtin_bit_cast(bf16x2, a), __builtin_bit_cast(bf16x2, b), c, false);
}
template <int CTRL> __device__ __forceinline__ float dpp(float x) {
  return __builtin_bit_cast(float, __builtin_amdgcn_mov_dpp(__builtin_bit_cast(int, x), CTRL, 0xf, 0xf, true));
}
constexpr int XOR1 = 0xB1, XOR2 = 0x4E, HMIRROR = 0x141, ROR8 = 0x128, ROR4 = 0x124, NEWBCAST = 0x150;

__device__ __forceinline__ float xor16_sum(float x) {
  auto s = __builtin_amdgcn_permlane16_swap(__float_as_uint(x), __float_as_uint(x), false, false);
  return __uint_as_float(s[0]) + __uint_as_float(s[1]);
}
__device__ __forceinline__ float xor32_sum(float x) {
  auto s = __builtin_amdgcn_permlane32_swap(__float_as_uint(x), __float_as_uint(x), false, false);
  return __uint_as_float(s[0]) + __uint_as_float(s[1]);
}
__device__ __forceinline__ float xor32_max(float x) {
  auto s = __builtin_amdgcn_permlane32_swap(__float_as_uint(x), __float_as_uint(x), false, false);
  return fmaxf(__uint_as_float(s[0]), __uint_as_float(s[1]));
}

struct Tile { u32x4 r[8]; };

__device__ __forceinline__ void issue(Tile& t, __amdgpu_buffer_rsrc_t rs, unsigned voff, unsigned soff) {
#pragma unroll
  for (int j = 0; j < 8; ++j)
  {
    t.r[j] = __builtin_amdgcn_raw_buffer_load_b128(rs, voff, soff + j * 2 * TOK_BYTES, KV_AUX);
    __builtin_amdgcn_sched_barrier(0);
  }
}

template <int J> __device__ __forceinline__ void pv_step(const u32x4& v, float p, float (&acc)[2][8]) {
  const float p0 = dpp<NEWBCAST + 2 * J>(p), p1 = dpp<NEWBCAST + 2 * J + 1>(p);
#pragma unroll
  for (int k = 0; k < 4; ++k) {
    const float lo = __uint_as_float(v[k] << 16), hi = __uint_as_float(v[k] & 0xffff0000u);
    acc[0][2 * k] = fmaf(p0, lo, acc[0][2 * k]);
    acc[0][2 * k + 1] = fmaf(p0, hi, acc[0][2 * k + 1]);
    acc[1][2 * k] = fmaf(p1, lo, acc[1][2 * k]);
    acc[1][2 * k + 1] = fmaf(p1, hi, acc[1][2 * k + 1]);
  }
}

__global__ void __launch_bounds__(NW * 64, (NW * NSPLIT * HKV) / 1024)
attn_split(const u32x4* __restrict__ q, const void* __restrict__ kc, const void* __restrict__ vc,
           const int* __restrict__ bt, const int* __restrict__ lens, float* __restrict__ ws,
           int nbps, unsigned kv_bytes) {
  __shared__ __attribute__((aligned(16))) float lds[NW * G * (D + 4)];
  const int head = blockIdx.x & (HKV - 1), s = blockIdx.x >> 3;
  const int lane = threadIdx.x & 63;
  const int w = __builtin_amdgcn_readfirstlane(threadIdx.x >> 6);
  const int wv = w * NSPLIT + s;
  const int h = lane >> 5, c = lane & 31;

  int btv = 0;
  {
    const int pg = wv + lane * T;
    if (pg < nbps) btv = bt[pg];
  }
  const int len_raw = lens[0];
  u32x4 qv[G];
#pragma unroll
  for (int g = 0; g < G; ++g) qv[g] = q[(head * G + g) * (D / 8) + c];

  const int skv = nbps * PAGE;
  const bool all_masked = len_raw <= 0;
  const int len = all_masked ? skv : min(len_raw, skv);
  const float sc = all_masked ? 0.f : 0.0625f * 1.4426950408889634f;
  const int npages = (len + PAGE - 1) / PAGE;
  const int n = npages > wv ? (npages - wv + T - 1) / T : 0;

  const __amdgpu_buffer_rsrc_t krs = __builtin_amdgcn_make_buffer_rsrc((void*)kc, 0, kv_bytes, 0x00020000);
  const __amdgpu_buffer_rsrc_t vrs = __builtin_amdgcn_make_buffer_rsrc((void*)vc, 0, kv_bytes, 0x00020000);
  const unsigned voff = h * TOK_BYTES + c * 16;
  const unsigned hoff = head * (D * 2);
  auto page_off = [&](int i) -> unsigned {
    const unsigned phys = (unsigned)__builtin_amdgcn_readlane(btv, i & 63);
    return i < n ? phys * PAGE_BYTES + hoff : 0xF0000000u;
  };

  float m = NEG_BIG, l = 0.f, acc[2][8];
#pragma unroll
  for (int k = 0; k < 8; ++k) { acc[0][k] = 0.f; acc[1][k] = 0.f; }
  const int tok_in_page = 2 * ((lane & 15) >> 1) + h;

  Tile KA, VA, KB, VB;
  {
    const unsigned o0 = page_off(0), o1 = page_off(1);
    issue(KA, krs, voff, o0);
    issue(VA, vrs, voff, o0);
    issue(KB, krs, voff, o1);
    issue(VB, vrs, voff, o1);
  }

  auto body = [&](Tile& K, Tile& V, int i) {
    float x[16];
#pragma unroll
    for (int j = 0; j < 8; ++j)
#pragma unroll
      for (int g = 0; g < G; ++g) {
        float d = 0.f;
#pragma unroll
        for (int k = 0; k < 4; ++k) d = dot2(K.r[j][k], qv[g][k], d);
        x[2 * j + g] = d;
      }
    const unsigned onext = page_off(i + 2);
    __builtin_amdgcn_sched_barrier(0);
    issue(K, krs, voff, onext);
    __builtin_amdgcn_sched_barrier(0);
#define TR_STEP(HALF, CTRL, BIT)                                            \
  _Pragma("unroll") for (int u = 0; u < (HALF); ++u) {                      \
    const float lo_ = x[u], hi_ = x[u + (HALF)];                            \
    const float t1_ = lo_ + dpp<CTRL>(lo_), t2_ = hi_ + dpp<CTRL>(hi_);     \
    x[u] = (lane & (BIT)) ? t2_ : t1_;                                      \
  }
    TR_STEP(8, ROR8, 8) TR_STEP(4, HMIRROR, 4) TR_STEP(2, XOR2, 2) TR_STEP(1, XOR1, 1)
#undef TR_STEP
    float sv = xor16_sum(x[0]) * sc;
    const int tok = (wv + i * T) * PAGE + tok_in_page;
    sv = tok < len ? sv : -INFINITY;
    float pm = fmaxf(sv, dpp<XOR2>(sv));
    pm = fmaxf(pm, dpp<ROR4>(pm));
    pm = fmaxf(pm, dpp<ROR8>(pm));
    const float mn = fmaxf(m, pm);
    if (__builtin_amdgcn_ballot_w64(mn > m)) {
      const float alpha = __builtin_amdgcn_exp2f(m - mn);
      l *= alpha;
      const float a0 = dpp<0x00>(alpha), a1 = dpp<0x55>(alpha);
#pragma unroll
      for (int k = 0; k < 8; ++k) { acc[0][k] *= a0; acc[1][k] *= a1; }
      m = mn;
    }
    const float p = __builtin_amdgcn_exp2f(sv - m);
    l += p;
    pv_step<0>(V.r[0], p, acc); pv_step<1>(V.r[1], p, acc);
    pv_step<2>(V.r[2], p, acc); pv_step<3>(V.r[3], p, acc);
    pv_step<4>(V.r[4], p, acc); pv_step<5>(V.r[5], p, acc);
    pv_step<6>(V.r[6], p, acc); pv_step<7>(V.r[7], p, acc);
    __builtin_amdgcn_sched_barrier(0);
    issue(V, vrs, voff, onext);
    __builtin_amdgcn_sched_barrier(0);
  };

  for (int i = 0; i < n; i += 2) {
    body(KA, VA, i);
    if (i + 1 >= n) break;
    body(KB, VB, i + 1);
  }

  const float M = xor32_max(m);
  const float f = __builtin_amdgcn_exp2f(m - M);
  l *= f;
  l += dpp<XOR2>(l);
  l += dpp<ROR4>(l);
  l += dpp<ROR8>(l);
  l = xor32_sum(l);
  const float f0 = dpp<0x00>(f), f1 = dpp<0x55>(f);
#pragma unroll
  for (int k = 0; k < 8; ++k) {
    acc[0][k] = xor32_sum(acc[0][k] * f0);
    acc[1][k] = xor32_sum(acc[1][k] * f1);
  }
  {
    float* rec = lds + (w * G + h) * (D + 4);
    f32x4 a, b;
#pragma unroll
    for (int k = 0; k < 4; ++k) { a[k] = h ? acc[1][k] : acc[0][k]; b[k] = h ? acc[1][4 + k] : acc[0][4 + k]; }
    *reinterpret_cast<f32x4*>(rec + 4 + 8 * c) = a;
    *reinterpret_cast<f32x4*>(rec + 8 + 8 * c) = b;
    if (lane < 2) { float* r2 = lds + (w * G + lane) * (D + 4); r2[0] = M; r2[1] = l; }
  }
  __syncthreads();
  for (int t = threadIdx.x; t < G * D; t += NW * 64) {
    const int g = t >> 8, d = t & (D - 1);
    float mw[NW], Mx = NEG_BIG;
#pragma unroll
    for (int u = 0; u < NW; ++u) { mw[u] = lds[(u * G + g) * (D + 4)]; Mx = fmaxf(Mx, mw[u]); }
    float o = 0.f, L = 0.f;
#pragma unroll
    for (int u = 0; u < NW; ++u) {
      const float wt = __builtin_amdgcn_exp2f(mw[u] - Mx);
      o = fmaf(wt, lds[(u * G + g) * (D + 4) + 4 + d], o);
      L = fmaf(wt, lds[(u * G + g) * (D + 4) + 1], L);
    }
    const int row = head * G + g;
    ws[(row * NSPLIT + s) * D + d] = o;
    if (d == 0) {
      float* pml = ws + HKV * G * NSPLIT * D + (row * NSPLIT + s) * 2;
      *reinterpret_cast<f32x2*>(pml) = f32x2{Mx, L};
    }
  }
}

__global__ void __launch_bounds__(256) attn_combine(const float* __restrict__ ws, __hip_bfloat16* __restrict__ out) {
  const int row = blockIdx.x, d = threadIdx.x;
  const f32x2* pml = reinterpret_cast<const f32x2*>(ws + HKV * G * NSPLIT * D) + row * NSPLIT;
  const float* po = ws + row * NSPLIT * D + d;
  float ov[NSPLIT];
  f32x2 ml[NSPLIT];
#pragma unroll
  for (int s = 0; s < NSPLIT; ++s) { ov[s] = po[s * D]; ml[s] = pml[s]; }
  float M = NEG_BIG;
#pragma unroll
  for (int s = 0; s < NSPLIT; ++s) M = fmaxf(M, ml[s][0]);
  float o = 0.f, L = 0.f;
#pragma unroll
  for (int s = 0; s < NSPLIT; ++s) {
    const float wt = __builtin_amdgcn_exp2f(ml[s][0] - M);
    o = fmaf(wt, ov[s], o);
    L = fmaf(wt, ml[s][1], L);
  }
  out[row * D + d] = __float2bfloat16(o / L);
}

}

extern "C" void kernel_launch(void* const* d_in, const int* in_sizes, int n_in, void* d_out, int out_size,
                              void* d_ws, size_t ws_size, hipStream_t stream) {
  const int nbps = in_sizes[3];
  const unsigned kv_bytes = (unsigned)in_sizes[1] * 2u;
  attn_split<<<HKV * NSPLIT, NW * 64, 0, stream>>>((const u32x4*)d_in[0], d_in[1], d_in[2], (const int*)d_in[3],
                                                   (const int*)d_in[4], (float*)d_ws, nbps, kv_bytes);
  attn_combine<<<HKV * G, 256, 0, stream>>>((const float*)d_ws, (__hip_bfloat16*)d_out);
}
```

```cpp
#include <hip/hip_runtime.h>
#include <hip/hip_bf16.h>
#include <stdint.h>

#define STAMP(i)
namespace {

constexpr int D = 256, PAGE = 16, HKV = 8, G = 2;
#define NW 4
#define NSPLIT 32
#define MAPMODE 0
#define MINW ((NW * NSPLIT * 8) / 1024)
#define KV_AUX 2
constexpr int T = NW * NSPLIT;
constexpr unsigned PAGE_BYTES = PAGE * HKV * D * 2;
constexpr unsigned TOK_BYTES = HKV * D * 2;
constexpr float NEG_BIG = -3.0e38f;

typedef unsigned u32x4 __attribute__((ext_vector_type(4)));
typedef float f32x4 __attribute__((ext_vector_type(4)));
typedef float f32x2 __attribute__((ext_vector_type(2)));
typedef __bf16 bf16x2 __attribute__((ext_vector_type(2)));

__device__ __forceinline__ float dot2(unsigned a, unsigned b, float c) {
  return __builtin_amdgcn_fdot2_f32_bf16(__builtin_bit_cast(bf16x2, a), __builtin_bit_cast(bf16x2, b), c, false);
}
template <int CTRL> __device__ __forceinline__ float dpp(float x) {
  return __builtin_bit_cast(float, __builtin_amdgcn_mov_dpp(__builtin_bit_cast(int, x), CTRL, 0xf, 0xf, true));
}
constexpr int XOR1 = 0xB1, XOR2 = 0x4E, HMIRROR = 0x141, ROR8 = 0x128, ROR4 = 0x124, NEWBCAST = 0x150;

__device__ __forceinline__ float xor16_sum(float x) {
  auto s = __builtin_amdgcn_permlane16_swap(__float_as_uint(x), __float_as_uint(x), false, false);
  return __uint_as_float(s[0]) + __uint_as_float(s[1]);
}
__device__ __forceinline__ float xor32_sum(float x) {
  auto s = __builtin_amdgcn_permlane32_swap(__float_as_uint(x), __float_as_uint(x), false, false);
  return __uint_as_float(s[0]) + __uint_as_float(s[1]);
}
__device__ __forceinline__ float xor32_max(float x) {
  auto s = __builtin_amdgcn_permlane32_swap(__float_as_uint(x), __float_as_uint(x), false, false);
  return fmaxf(__uint_as_float(s[0]), __uint_as_float(s[1]));
}

#define PIN() do { asm volatile("" ::: "memory"); __builtin_amdgcn_sched_barrier(0); } while (0)
struct Tile { u32x4 r[8]; };

__device__ __forceinline__ void issue(Tile& t, __amdgpu_buffer_rsrc_t rs, unsigned voff, unsigned soff) {
#pragma unroll
  for (int j = 0; j < 8; ++j)
  {
    t.r[j] = __builtin_amdgcn_raw_buffer_load_b128(rs, voff, soff + j * 2 * TOK_BYTES, KV_AUX);
    __builtin_amdgcn_sched_barrier(0);
  }
}

template <int J> __device__ __forceinline__ void pv_step(const u32x4& v, float p, float (&acc)[2][8]) {
  const float p0 = dpp<NEWBCAST + 2 * J>(p), p1 = dpp<NEWBCAST + 2 * J + 1>(p);
#pragma unroll
  for (int k = 0; k < 4; ++k) {
    const float lo = __uint_as_float(v[k] << 16), hi = __uint_as_float(v[k] & 0xffff0000u);
    acc[0][2 * k] = fmaf(p0, lo, acc[0][2 * k]);
    acc[0][2 * k + 1] = fmaf(p0, hi, acc[0][2 * k + 1]);
    acc[1][2 * k] = fmaf(p1, lo, acc[1][2 * k]);
    acc[1][2 * k + 1] = fmaf(p1, hi, acc[1][2 * k + 1]);
  }
  __builtin_amdgcn_sched_barrier(0);
}

__global__ void __launch_bounds__(NW * 64, MINW)
attn_split(const u32x4* __restrict__ q, const void* __restrict__ kc, const void* __restrict__ vc,
           const int* __restrict__ bt, const int* __restrict__ lens, float* __restrict__ ws,
           int nbps, unsigned kv_bytes) {
  __shared__ __attribute__((aligned(16))) float lds[NW * G * (D + 4)];
  const int head = blockIdx.x & (HKV - 1), s = blockIdx.x >> 3;
  const int lane = threadIdx.x & 63;
  const int w = __builtin_amdgcn_readfirstlane(threadIdx.x >> 6);
  const int wv = w * NSPLIT + s;
  const int h = lane >> 5, c = lane & 31;

  int btv = 0;
  {
    const int pg = wv + lane * T;
    if (pg < nbps) btv = bt[pg];
  }
  const int len_raw = lens[0];
  u32x4 qv[G];
#pragma unroll
  for (int g = 0; g < G; ++g) qv[g] = q[(head * G + g) * (D / 8) + c];

  const int skv = nbps * PAGE;
  const bool all_masked = len_raw <= 0;
  const int len = all_masked ? skv : min(len_raw, skv);
  const float sc = all_masked ? 0.f : 0.0625f * 1.4426950408889634f;
  const int npages = (len + PAGE - 1) / PAGE;
  const int n = npages > wv ? (npages - wv + T - 1) / T : 0;

  const __amdgpu_buffer_rsrc_t krs = __builtin_amdgcn_make_buffer_rsrc((void*)kc, 0, kv_bytes, 0x00020000);
  const __amdgpu_buffer_rsrc_t vrs = __builtin_amdgcn_make_buffer_rsrc((void*)vc, 0, kv_bytes, 0x00020000);
  const unsigned voff = h * TOK_BYTES + c * 16;
  const unsigned hoff = head * (D * 2);
  auto page_off = [&](int i) -> unsigned {
    const unsigned phys = (unsigned)__builtin_amdgcn_readlane(btv, i & 63);
    return i < n ? phys * PAGE_BYTES + hoff : 0xF0000000u;
  };

  float m = NEG_BIG, l = 0.f, acc[2][8];
#pragma unroll
  for (int k = 0; k < 8; ++k) { acc[0][k] = 0.f; acc[1][k] = 0.f; }
  const int tok_in_page = 2 * ((lane & 15) >> 1) + h;

  Tile KA, VA;
  {
    const unsigned o0 = page_off(0);
    issue(KA, krs, voff, o0);
    issue(VA, vrs, voff, o0);
    PIN();
  }

  auto body = [&](Tile& K, Tile& V, int i, bool more) {
    float x[16];
#pragma unroll
    for (int j = 0; j < 8; ++j)
#pragma unroll
      for (int g = 0; g < G; ++g) {
        float d = 0.f;
#pragma unroll
        for (int k = 0; k < 4; ++k) d = dot2(K.r[j][k], qv[g][k], d);
        x[2 * j + g] = d;
      }
    const unsigned onext = page_off(i + 1);
    PIN();
    if (more) issue(K, krs, voff, onext);
    PIN();
#define TR_STEP(HALF, CTRL, BIT)                                            \
  _Pragma("unroll") for (int u = 0; u < (HALF); ++u) {                      \
    const float lo_ = x[u], hi_ = x[u + (HALF)];                            \
    const float t1_ = lo_ + dpp<CTRL>(lo_), t2_ = hi_ + dpp<CTRL>(hi_);     \
    x[u] = (lane & (BIT)) ? t2_ : t1_;                                      \
  }
    TR_STEP(8, ROR8, 8) TR_STEP(4, HMIRROR, 4) TR_STEP(2, XOR2, 2) TR_STEP(1, XOR1, 1)
#undef TR_STEP
    float sv = xor16_sum(x[0]) * sc;
    const int tok = (wv + i * T) * PAGE + tok_in_page;
    sv = tok < len ? sv : -INFINITY;
    float pm = fmaxf(sv, dpp<XOR2>(sv));
    pm = fmaxf(pm, dpp<ROR4>(pm));
    pm = fmaxf(pm, dpp<ROR8>(pm));
    const float mn = fmaxf(m, pm);
    if (__builtin_amdgcn_ballot_w64(mn > m)) {
      const float alpha = __builtin_amdgcn_exp2f(m - mn);
      l *= alpha;
      const float a0 = dpp<0x00>(alpha), a1 = dpp<0x55>(alpha);
#pragma unroll
      for (int k = 0; k < 8; ++k) { acc[0][k] *= a0; acc[1][k] *= a1; }
      m = mn;
    }
    const float p = __builtin_amdgcn_exp2f(sv - m);
    l += p;
    pv_step<0>(V.r[0], p, acc); pv_step<1>(V.r[1], p, acc);
    pv_step<2>(V.r[2], p, acc); pv_step<3>(V.r[3], p, acc);
    pv_step<4>(V.r[4], p, acc); pv_step<5>(V.r[5], p, acc);
    pv_step<6>(V.r[6], p, acc); pv_step<7>(V.r[7], p, acc);
    PIN();
    if (more) issue(V, vrs, voff, onext);
    PIN();
  };

  { int i = 0; do { body(KA, VA, i, true); } while (++i < n); }

  const float M = xor32_max(m);
  const float f = __builtin_amdgcn_exp2f(m - M);
  l *= f;
  l += dpp<XOR2>(l);
  l += dpp<ROR4>(l);
  l += dpp<ROR8>(l);
  l = xor32_sum(l);
  const float f0 = dpp<0x00>(f), f1 = dpp<0x55>(f);
#pragma unroll
  for (int k = 0; k < 8; ++k) {
    acc[0][k] = xor32_sum(acc[0][k] * f0);
    acc[1][k] = xor32_sum(acc[1][k] * f1);
  }
  {
    float* rec = lds + (w * G + h) * (D + 4);
    f32x4 a, b;
#pragma unroll
    for (int k = 0; k < 4; ++k) { a[k] = h ? acc[1][k] : acc[0][k]; b[k] = h ? acc[1][4 + k] : acc[0][4 + k]; }
    *reinterpret_cast<f32x4*>(rec + 4 + 8 * c) = a;
    *reinterpret_cast<f32x4*>(rec + 8 + 8 * c) = b;
    if (lane < 2) { float* r2 = lds + (w * G + lane) * (D + 4); r2[0] = M; r2[1] = l; }
  }
  __syncthreads();
  for (int t = threadIdx.x; t < G * D; t += NW * 64) {
    const int g = t >> 8, d = t & (D - 1);
    float mw[NW], Mx = NEG_BIG;
#pragma unroll
    for (int u = 0; u < NW; ++u) { mw[u] = lds[(u * G + g) * (D + 4)]; Mx = fmaxf(Mx, mw[u]); }
    float o = 0.f, L = 0.f;
#pragma unroll
    for (int u = 0; u < NW; ++u) {
      const float wt = __builtin_amdgcn_exp2f(mw[u] - Mx);
      o = fmaf(wt, lds[(u * G + g) * (D + 4) + 4 + d], o);
      L = fmaf(wt, lds[(u * G + g) * (D + 4) + 1], L);
    }
    const int row = head * G + g;
    ws[(row * NSPLIT + s) * D + d] = o;
    if (d == 0) {
      float* pml = ws + HKV * G * NSPLIT * D + (row * NSPLIT + s) * 2;
      *reinterpret_cast<f32x2*>(pml) = f32x2{Mx, L};
    }
  }
}

__global__ void __launch_bounds__(256) attn_combine(const float* __restrict__ ws, __hip_bfloat16* __restrict__ out) {
  const int row = blockIdx.x, d = threadIdx.x;
  const f32x2* pml = reinterpret_cast<const f32x2*>(ws + HKV * G * NSPLIT * D) + row * NSPLIT;
  const float* po = ws + row * NSPLIT * D + d;
  float ov[NSPLIT];
  f32x2 ml[NSPLIT];
#pragma unroll
  for (int s = 0; s < NSPLIT; ++s) { ov[s] = po[s * D]; ml[s] = pml[s]; }
  float M = NEG_BIG;
#pragma unroll
  for (int s = 0; s < NSPLIT; ++s) M = fmaxf(M, ml[s][0]);
  float o = 0.f, L = 0.f;
#pragma unroll
  for (int s = 0; s < NSPLIT; ++s) {
    const float wt = __builtin_amdgcn_exp2f(ml[s][0] - M);
    o = fmaf(wt, ov[s], o);
    L = fmaf(wt, ml[s][1], L);
  }
  out[row * D + d] = __float2bfloat16(o / L);
}

}

extern "C" void kernel_launch(void* const* d_in, const int* in_sizes, int n_in, void* d_out, int out_size,
                              void* d_ws, size_t ws_size, hipStream_t stream) {
  const int nbps = in_sizes[3];
  const unsigned kv_bytes = (unsigned)in_sizes[1] * 2u;
  attn_split<<<HKV * NSPLIT, NW * 64, 0, stream>>>((const u32x4*)d_in[0], d_in[1], d_in[2], (const int*)d_in[3],
                                                   (const int*)d_in[4], (float*)d_ws, nbps, kv_bytes);
  attn_combine<<<HKV * G, 256, 0, stream>>>((const float*)d_ws, (__hip_bfloat16*)d_out);
}
```

```cpp
#include <hip/hip_runtime.h>
#include <hip/hip_bf16.h>
#include <stdint.h>

#define NW 4
#define LBW 2
#define GATE 32
#define GATESLEEP 2
#define TLBWARM 1

namespace {

constexpr int D = 256, PAGE = 16, HKV = 8, G = 2, NSPLIT = 32;
constexpr int T = NW * NSPLIT;
constexpr unsigned PAGE_BYTES = PAGE * HKV * D * 2;
constexpr unsigned TOK_BYTES = HKV * D * 2;
constexpr float NEG_BIG = -3.0e38f;
constexpr int AUX_NT = 2, AUX_SC1 = 16;
constexpr unsigned SENT = 0xFFFFFFFFu;
constexpr unsigned WS_E = 0, WS_F = 4096, WS_P = WS_F + HKV * NSPLIT * 128, WS_BYTES = WS_P + HKV * NSPLIT * G * D * 4;
constexpr unsigned OOB = 0xF0000000u;
constexpr int REC = D + 4;
constexpr unsigned SPIN_CAP = 400000;

typedef unsigned u32x4 __attribute__((ext_vector_type(4)));
typedef unsigned u32x2 __attribute__((ext_vector_type(2)));
typedef float f32x4 __attribute__((ext_vector_type(4)));
typedef float f32x2 __attribute__((ext_vector_type(2)));
typedef __bf16 bf16x2 __attribute__((ext_vector_type(2)));

__device__ __forceinline__ float dot2(unsigned a, unsigned b, float c) {
  return __builtin_amdgcn_fdot2_f32_bf16(__builtin_bit_cast(bf16x2, a), __builtin_bit_cast(bf16x2, b), c, false);
}
template <int CTRL> __device__ __forceinline__ float dpp(float x) {
  return __builtin_bit_cast(float, __builtin_amdgcn_mov_dpp(__builtin_bit_cast(int, x), CTRL, 0xf, 0xf, true));
}
constexpr int XOR1 = 0xB1, XOR2 = 0x4E, HMIRROR = 0x141, ROR8 = 0x128, ROR4 = 0x124, NEWBCAST = 0x150;

__device__ __forceinline__ float xor16_sum(float x) {
  auto s = __builtin_amdgcn_permlane16_swap(__float_as_uint(x), __float_as_uint(x), false, false);
  return __uint_as_float(s[0]) + __uint_as_float(s[1]);
}
__device__ __forceinline__ float xor16_max(float x) {
  auto s = __builtin_amdgcn_permlane16_swap(__float_as_uint(x), __float_as_uint(x), false, false);
  return fmaxf(__uint_as_float(s[0]), __uint_as_float(s[1]));
}
__device__ __forceinline__ float sum32(float x) {
  x += dpp<XOR1>(x); x += dpp<XOR2>(x); x += dpp<HMIRROR>(x); x += dpp<ROR8>(x);
  return xor16_sum(x);
}
__device__ __forceinline__ float max32(float x) {
  x = fmaxf(x, dpp<XOR1>(x)); x = fmaxf(x, dpp<XOR2>(x)); x = fmaxf(x, dpp<HMIRROR>(x)); x = fmaxf(x, dpp<ROR8>(x));
  return xor16_max(x);
}
__device__ __forceinline__ unsigned pack_bf16(float lo, float hi) {
  const bf16x2 v = {(__bf16)lo, (__bf16)hi};
  return __builtin_bit_cast(unsigned, v);
}
__device__ __forceinline__ unsigned not_sent(float x) { return min(__float_as_uint(x), SENT - 1u); }

#define PIN() do { asm volatile("" ::: "memory"); __builtin_amdgcn_sched_barrier(0); } while (0)
struct Tile { u32x4 r[8]; };

__device__ __forceinline__ void issue(Tile& t, __amdgpu_buffer_rsrc_t rs, unsigned voff, unsigned soff) {
#pragma unroll
  for (int j = 0; j < 8; ++j) {
    t.r[j] = __builtin_amdgcn_raw_buffer_load_b128(rs, voff, soff + j * 2 * TOK_BYTES, AUX_NT);
    __builtin_amdgcn_sched_barrier(0);
  }
}

template <int J> __device__ __forceinline__ void pv_step(const u32x4& v, float p, float (&acc)[2][8]) {
  const float p0 = dpp<NEWBCAST + 2 * J>(p), p1 = dpp<NEWBCAST + 2 * J + 1>(p);
#pragma unroll
  for (int k = 0; k < 4; ++k) {
    const float lo = __uint_as_float(v[k] << 16), hi = __uint_as_float(v[k] & 0xffff0000u);
    acc[0][2 * k] = fmaf(p0, lo, acc[0][2 * k]);
    acc[0][2 * k + 1] = fmaf(p0, hi, acc[0][2 * k + 1]);
    acc[1][2 * k] = fmaf(p1, lo, acc[1][2 * k]);
    acc[1][2 * k + 1] = fmaf(p1, hi, acc[1][2 * k + 1]);
  }
  __builtin_amdgcn_sched_barrier(0);
}

__global__ void __launch_bounds__((NW + 1) * 64, LBW)
attn_decode(const u32x4* __restrict__ q, const void* __restrict__ kc, const void* __restrict__ vc,
            const int* __restrict__ bt, const int* __restrict__ lens, unsigned char* __restrict__ ws,
            unsigned char* __restrict__ out, int nbps, unsigned kv_bytes) {
  __shared__ __attribute__((aligned(16))) float lds[NW * 2 * G * REC];
  asm volatile("" ::"s"(q), "s"(kc), "s"(vc), "s"(bt), "s"(lens), "s"(ws), "s"(out), "s"(nbps), "s"(kv_bytes));
  const int head = blockIdx.x & (HKV - 1), s = blockIdx.x >> 3;
  const int lane = threadIdx.x & 63;
  const int w = __builtin_amdgcn_readfirstlane(threadIdx.x >> 6);
  const int h = lane >> 5, c = lane & 31;
  const __amdgpu_buffer_rsrc_t krs = __builtin_amdgcn_make_buffer_rsrc((void*)kc, 0, kv_bytes, 0x00020000);
  const __amdgpu_buffer_rsrc_t vrs = __builtin_amdgcn_make_buffer_rsrc((void*)vc, 0, kv_bytes, 0x00020000);

  if (w < NW) {
    const int wv = w * NSPLIT + s;
    const int btv = bt[min(wv + lane * T, nbps - 1)];
    const int len_raw = lens[0];
    u32x4 qv[G];
#pragma unroll
    for (int g = 0; g < G; ++g) qv[g] = q[(head * G + g) * (D / 8) + c];
    const int skv = nbps * PAGE;
    const bool all_masked = len_raw <= 0;
    const int len = all_masked ? skv : min(len_raw, skv);
    const float sc = all_masked ? 0.f : 0.0625f * 1.4426950408889634f;
    const int npages = (len + PAGE - 1) / PAGE;
    const int n = npages > wv ? (npages - wv + T - 1) / T : 0;
    const unsigned voff = h * TOK_BYTES + c * 16;
    const unsigned hoff = head * (D * 2);
    auto page_off = [&](int i) -> unsigned {
      const unsigned o = (unsigned)__builtin_amdgcn_readlane(btv, i & 63) * PAGE_BYTES + hoff;
      return i < n ? o : 0xF0000000u;
    };
    float m = NEG_BIG, l = 0.f, acc[2][8];
#pragma unroll
    for (int k = 0; k < 8; ++k) { acc[0][k] = 0.f; acc[1][k] = 0.f; }
    int tok = wv * PAGE + 2 * ((lane & 15) >> 1) + h;
    Tile K, V;
    {
      const unsigned o0 = page_off(0);
      issue(K, krs, voff, o0);
      issue(V, vrs, voff, o0);
      PIN();
    }
    int i = 0;
    do {
      float x[16];
#pragma unroll
      for (int j = 0; j < 8; ++j)
#pragma unroll
        for (int g = 0; g < G; ++g) {
          float d = 0.f;
#pragma unroll
          for (int k = 0; k < 4; ++k) d = dot2(K.r[j][k], qv[g][k], d);
          x[2 * j + g] = d;
        }
      ++i;
      const unsigned onext = page_off(i);
      PIN();
      issue(K, krs, voff, onext);
      PIN();
#define TR_STEP(HALF, CTRL, BIT)                                            \
  _Pragma("unroll") for (int u = 0; u < (HALF); ++u) {                      \
    const float lo_ = x[u], hi_ = x[u + (HALF)];                            \
    const float t1_ = lo_ + dpp<CTRL>(lo_), t2_ = hi_ + dpp<CTRL>(hi_);     \
    x[u] = (lane & (BIT)) ? t2_ : t1_;                                      \
  }
      TR_STEP(8, ROR8, 8) TR_STEP(4, HMIRROR, 4) TR_STEP(2, XOR2, 2) TR_STEP(1, XOR1, 1)
#undef TR_STEP
      float sv = xor16_sum(x[0]) * sc;
      sv = tok < len ? sv : -INFINITY;
      tok += T * PAGE;
      float pm = fmaxf(sv, dpp<XOR2>(sv));
      pm = fmaxf(pm, dpp<ROR4>(pm));
      pm = fmaxf(pm, dpp<ROR8>(pm));
      const float mn = fmaxf(m, pm);
      if (__builtin_amdgcn_ballot_w64(mn > m)) {
        const float alpha = __builtin_amdgcn_exp2f(m - mn);
        l *= alpha;
        const float a0 = dpp<0x00>(alpha), a1 = dpp<0x55>(alpha);
#pragma unroll
        for (int k = 0; k < 8; ++k) { acc[0][k] *= a0; acc[1][k] *= a1; }
        m = mn;
      }
      const float p = __builtin_amdgcn_exp2f(sv - m);
      l += p;
      pv_step<0>(V.r[0], p, acc); pv_step<1>(V.r[1], p, acc);
      pv_step<2>(V.r[2], p, acc); pv_step<3>(V.r[3], p, acc);
      pv_step<4>(V.r[4], p, acc); pv_step<5>(V.r[5], p, acc);
      pv_step<6>(V.r[6], p, acc); pv_step<7>(V.r[7], p, acc);
      PIN();
      issue(V, vrs, voff, onext);
      PIN();
    } while (i < n);
    l += dpp<XOR2>(l);
    l += dpp<ROR4>(l);
    l += dpp<ROR8>(l);
#pragma unroll
    for (int g = 0; g < G; ++g) {
      float* rec = lds + ((w * 2 + h) * G + g) * REC;
      *reinterpret_cast<f32x4*>(rec + 4 + 8 * c) = f32x4{acc[g][0], acc[g][1], acc[g][2], acc[g][3]};
      *reinterpret_cast<f32x4*>(rec + 8 + 8 * c) = f32x4{acc[g][4], acc[g][5], acc[g][6], acc[g][7]};
    }
    if (c < 2) *reinterpret_cast<f32x2*>(lds + ((w * 2 + h) * G + c) * REC) = f32x2{m, l};
    __syncthreads();
    return;
  }

  const unsigned woff = lane < 16 ? ((unsigned)(s * 8 + (lane & 7)) << 20) + ((lane >> 3) << 28) : 0xF0000000u;
  const unsigned wa = __builtin_amdgcn_raw_buffer_load_b32(krs, woff, 0, AUX_NT);
  const unsigned wb = __builtin_amdgcn_raw_buffer_load_b32(vrs, woff, 0, AUX_NT);
  PIN();
  const __amdgpu_buffer_rsrc_t wrs = __builtin_amdgcn_make_buffer_rsrc(ws, 0, WS_BYTES, 0x00020000);
  const unsigned slot = WS_P + (head * NSPLIT + s) * (G * D * 4);
  const unsigned put_f = lane == 0 ? WS_F + (head * NSPLIT + s) * 128 : OOB;
  const unsigned get_f = WS_F + (head * NSPLIT + c) * 128;
  const u32x2 e = __builtin_amdgcn_raw_buffer_load_b64(wrs, 0, WS_E + head * 256, AUX_SC1);
  {
    const u32x4 sent4 = {SENT, SENT, SENT, SENT};
    __builtin_amdgcn_raw_buffer_store_b128(sent4, wrs, lane * 16, slot, AUX_SC1);
    __builtin_amdgcn_raw_buffer_store_b128(sent4, wrs, lane * 16 + 1024, slot, AUX_SC1);
    __builtin_amdgcn_raw_buffer_store_b64(u32x2{SENT, SENT}, wrs, put_f + 8, 0, AUX_SC1);
  }
  asm volatile("" ::"v"(wa), "v"(wb));
  const unsigned long long enext = (((unsigned long long)e[1] << 32) | e[0]) + 1ull;
  const unsigned fa_lo = (unsigned)(enext << 1), fa_hi = (unsigned)(enext >> 31);
  asm volatile("s_waitcnt vmcnt(0)" ::: "memory");
  __builtin_amdgcn_raw_buffer_store_b64(u32x2{fa_lo, fa_hi}, wrs, put_f, 0, AUX_SC1);
  const int g_out = s >> 4;
  const unsigned get = (c == s) ? OOB : WS_P + ((head * NSPLIT + c) * G + g_out) * (D * 4) + (16 * (s & 15) + 8 * h) * 4;
  const unsigned get_l = get_f + 8 + g_out * 4;
  __syncthreads();
  u32x2 f = __builtin_amdgcn_raw_buffer_load_b64(wrs, get_f, 0, AUX_SC1);
  PIN();
  u32x4 pa, pb;
  unsigned pl;
  {
    f32x2 ml[G][2 * NW];
    f32x4 ra[G][2 * NW];
#pragma unroll
    for (int g = 0; g < G; ++g)
#pragma unroll
      for (int r = 0; r < 2 * NW; ++r) ml[g][r] = *reinterpret_cast<const f32x2*>(lds + (r * G + g) * REC);
#pragma unroll
    for (int g = 0; g < G; ++g)
#pragma unroll
      for (int r = 0; r < 2 * NW; ++r) ra[g][r] = *reinterpret_cast<const f32x4*>(lds + (r * G + g) * REC + 4 + 4 * lane);
    __builtin_amdgcn_sched_barrier(0);
    u32x4 uo[G];
    unsigned ul[G];
#pragma unroll
    for (int g = 0; g < G; ++g) {
      float M = NEG_BIG;
#pragma unroll
      for (int r = 0; r < 2 * NW; ++r) M = fmaxf(M, ml[g][r][0]);
      float L = 0.f;
      f32x4 a = {0.f, 0.f, 0.f, 0.f};
#pragma unroll
      for (int r = 0; r < 2 * NW; ++r) {
        const float wt = __builtin_amdgcn_exp2f(ml[g][r][0] - M);
        L = fmaf(wt, ml[g][r][1], L);
        a += wt * ra[g][r];
      }
      const float inv = L > 0.f ? __builtin_amdgcn_rcpf(L) : 0.f;
      const float lse = L > 0.f ? M + __builtin_amdgcn_logf(L) : NEG_BIG;
      a *= inv;
      uo[g] = u32x4{not_sent(a[0]), not_sent(a[1]), not_sent(a[2]), not_sent(a[3])};
      ul[g] = not_sent(lse);
    }
    __builtin_amdgcn_raw_buffer_store_b128(uo[0], wrs, lane * 16, slot, AUX_SC1);
    __builtin_amdgcn_raw_buffer_store_b128(uo[1], wrs, lane * 16 + 1024, slot, AUX_SC1);
    __builtin_amdgcn_raw_buffer_store_b128(u32x4{fa_lo | 1u, fa_hi, ul[0], ul[1]}, wrs, put_f, 0, AUX_SC1);
    PIN();
    const u32x4 own = g_out ? uo[1] : uo[0];
    const int src = (4 * (s & 15) + 2 * h) * 4;
#pragma unroll
    for (int k = 0; k < 4; ++k) {
      pa[k] = __builtin_amdgcn_ds_bpermute(src, own[k]);
      pb[k] = __builtin_amdgcn_ds_bpermute(src + 4, own[k]);
    }
    pl = g_out ? ul[1] : ul[0];
  }
  {
    auto gate_closed = [&]() {
      const bool mine = (f[0] | 1u) == (fa_lo | 1u) && f[1] == fa_hi;
      const unsigned long long bad = __builtin_amdgcn_ballot_w64(!mine);
      const unsigned long long pub = __builtin_amdgcn_ballot_w64((f[0] & 1u) || c == s);
      return bad != 0 || __builtin_popcountll(pub) < 2 * GATE;
    };
    if (gate_closed()) {
      unsigned spin = 0;
      bool closed;
#pragma nounroll
      do {
        __builtin_amdgcn_s_sleep(GATESLEEP);
        asm volatile("" ::: "memory");
        if (!(f[0] == (fa_lo | 1u) && f[1] == fa_hi)) f = __builtin_amdgcn_raw_buffer_load_b64(wrs, get_f, 0, AUX_SC1);
        closed = gate_closed();
      } while (closed && ++spin < SPIN_CAP);
    }
    asm volatile("" ::: "memory");
    if (s == 0)
      __builtin_amdgcn_raw_buffer_store_b64(u32x2{(unsigned)enext, (unsigned)(enext >> 32)}, wrs, lane == 0 ? WS_E + head * 256 : OOB, 0, AUX_SC1);
    bool pending = c != s;
    unsigned spin = 0;
#pragma nounroll
    do {
      asm volatile("" ::: "memory");
      if (pending) {
        pa = __builtin_amdgcn_raw_buffer_load_b128(wrs, get, 0, AUX_SC1);
        pb = __builtin_amdgcn_raw_buffer_load_b128(wrs, get + 16, 0, AUX_SC1);
        pl = __builtin_amdgcn_raw_buffer_load_b32(wrs, get_l, 0, AUX_SC1);
        pending = max(max(max(pa[0], pa[1]), max(pa[2], pa[3])), max(max(max(pb[0], pb[1]), max(pb[2], pb[3])), pl)) == SENT;
      }
    } while (__builtin_amdgcn_ballot_w64(pending) != 0 && ++spin < SPIN_CAP);
    const float lse = __uint_as_float(pl);
    const float M = max32(lse);
    const float wt = __builtin_amdgcn_exp2f(lse - M);
    const float L = sum32(wt);
    float o[8];
#pragma unroll
    for (int k = 0; k < 4; ++k) {
      o[k] = sum32(wt * __uint_as_float(pa[k]));
      o[4 + k] = sum32(wt * __uint_as_float(pb[k]));
    }
    const float inv = 1.f / L;
    if (c == 0) {
      const u32x4 r = {pack_bf16(o[0] * inv, o[1] * inv), pack_bf16(o[2] * inv, o[3] * inv),
                       pack_bf16(o[4] * inv, o[5] * inv), pack_bf16(o[6] * inv, o[7] * inv)};
      *reinterpret_cast<u32x4*>(out + (head * G * D + 16 * s + 8 * h) * 2) = r;
    }
  }
}

}

extern "C" void kernel_launch(void* const* d_in, const int* in_sizes, int n_in, void* d_out, int out_size,
                              void* d_ws, size_t ws_size, hipStream_t stream) {
  const int nbps = in_sizes[3];
  const unsigned kv_bytes = (unsigned)in_sizes[1] * 2u;
  attn_decode<<<HKV * NSPLIT, (NW + 1) * 64, 0, stream>>>((const u32x4*)d_in[0], d_in[1], d_in[2], (const int*)d_in[3],
                                                          (const int*)d_in[4], (unsigned char*)d_ws,
                                                          (unsigned char*)d_out, nbps, kv_bytes);
}
```

```cpp
#include <hip/hip_runtime.h>
#include <hip/hip_bf16.h>
#include <stdint.h>

#define NW 4
#define LBW 2
#define GATE 32
#define GATESLEEP 2
#define TLBWARM 0

namespace {

constexpr int D = 256, PAGE = 16, HKV = 8, G = 2, NSPLIT = 32;
constexpr int T = NW * NSPLIT;
constexpr unsigned PAGE_BYTES = PAGE * HKV * D * 2;
constexpr unsigned TOK_BYTES = HKV * D * 2;
constexpr float NEG_BIG = -3.0e38f;
constexpr int AUX_NT = 2, AUX_SC1 = 16;
constexpr unsigned SENT = 0xFFFFFFFFu;
constexpr unsigned WS_E = 0, WS_F = 4096, WS_P = WS_F + HKV * NSPLIT * 128, WS_BYTES = WS_P + HKV * NSPLIT * G * D * 4;
constexpr unsigned OOB = 0xF0000000u;
constexpr int REC = D + 4;
constexpr unsigned SPIN_CAP = 400000;

typedef unsigned u32x4 __attribute__((ext_vector_type(4)));
typedef unsigned u32x2 __attribute__((ext_vector_type(2)));
typedef float f32x4 __attribute__((ext_vector_type(4)));
typedef float f32x2 __attribute__((ext_vector_type(2)));
typedef __bf16 bf16x2 __attribute__((ext_vector_type(2)));

__device__ __forceinline__ float dot2(unsigned a, unsigned b, float c) {
  return __builtin_amdgcn_fdot2_f32_bf16(__builtin_bit_cast(bf16x2, a), __builtin_bit_cast(bf16x2, b), c, false);
}
template <int CTRL> __device__ __forceinline__ float dpp(float x) {
  return __builtin_bit_cast(float, __builtin_amdgcn_mov_dpp(__builtin_bit_cast(int, x), CTRL, 0xf, 0xf, true));
}
constexpr int XOR1 = 0xB1, XOR2 = 0x4E, HMIRROR = 0x141, ROR8 = 0x128, ROR4 = 0x124, NEWBCAST = 0x150;

__device__ __forceinline__ float xor16_sum(float x) {
  auto s = __builtin_amdgcn_permlane16_swap(__float_as_uint(x), __float_as_uint(x), false, false);
  return __uint_as_float(s[0]) + __uint_as_float(s[1]);
}
__device__ __forceinline__ float xor16_max(float x) {
  auto s = __builtin_amdgcn_permlane16_swap(__float_as_uint(x), __float_as_uint(x), false, false);
  return fmaxf(__uint_as_float(s[0]), __uint_as_float(s[1]));
}
__device__ __forceinline__ float sum32(float x) {
  x += dpp<XOR1>(x); x += dpp<XOR2>(x); x += dpp<HMIRROR>(x); x += dpp<ROR8>(x);
  return xor16_sum(x);
}
__device__ __forceinline__ float max32(float x) {
  x = fmaxf(x, dpp<XOR1>(x)); x = fmaxf(x, dpp<XOR2>(x)); x = fmaxf(x, dpp<HMIRROR>(x)); x = fmaxf(x, dpp<ROR8>(x));
  return xor16_max(x);
}
__device__ __forceinline__ unsigned pack_bf16(float lo, float hi) {
  const bf16x2 v = {(__bf16)lo, (__bf16)hi};
  return __builtin_bit_cast(unsigned, v);
}
__device__ __forceinline__ unsigned not_sent(float x) { return min(__float_as_uint(x), SENT - 1u); }

#define PIN() do { asm volatile("" ::: "memory"); __builtin_amdgcn_sched_barrier(0); } while (0)
struct Tile { u32x4 r[8]; };

__device__ __forceinline__ void issue(Tile& t, __amdgpu_buffer_rsrc_t rs, unsigned voff, unsigned soff) {
#pragma unroll
  for (int j = 0; j < 8; ++j) {
    t.r[j] = __builtin_amdgcn_raw_buffer_load_b128(rs, voff, soff + j * 2 * TOK_BYTES, AUX_NT);
    __builtin_amdgcn_sched_barrier(0);
  }
}

template <int J> __device__ __forceinline__ void pv_step(const u32x4& v, float p, float (&acc)[2][8]) {
  const float p0 = dpp<NEWBCAST + 2 * J>(p), p1 = dpp<NEWBCAST + 2 * J + 1>(p);
#pragma unroll
  for (int k = 0; k < 4; ++k) {
    const float lo = __uint_as_float(v[k] << 16), hi = __uint_as_float(v[k] & 0xffff0000u);
    acc[0][2 * k] = fmaf(p0, lo, acc[0][2 * k]);
    acc[0][2 * k + 1] = fmaf(p0, hi, acc[0][2 * k + 1]);
    acc[1][2 * k] = fmaf(p1, lo, acc[1][2 * k]);
    acc[1][2 * k + 1] = fmaf(p1, hi, acc[1][2 * k + 1]);
  }
  __builtin_amdgcn_sched_barrier(0);
}

__global__ void __launch_bounds__((NW + 1) * 64, LBW)
attn_decode(const u32x4* __restrict__ q, const void* __restrict__ kc, const void* __restrict__ vc,
            const int* __restrict__ bt, const int* __restrict__ lens, unsigned char* __restrict__ ws,
            unsigned char* __restrict__ out, int nbps, unsigned kv_bytes) {
  __shared__ __attribute__((aligned(16))) float lds[NW * 2 * G * REC];
  asm volatile("" ::"s"(q), "s"(kc), "s"(vc), "s"(bt), "s"(lens), "s"(ws), "s"(out), "s"(nbps), "s"(kv_bytes));
  const int head = blockIdx.x & (HKV - 1), s = blockIdx.x >> 3;
  const int lane = threadIdx.x & 63;
  const int w = __builtin_amdgcn_readfirstlane(threadIdx.x >> 6);
  const int h = lane >> 5, c = lane & 31;
  const __amdgpu_buffer_rsrc_t krs = __builtin_amdgcn_make_buffer_rsrc((void*)kc, 0, kv_bytes, 0x00020000);
  const __amdgpu_buffer_rsrc_t vrs = __builtin_amdgcn_make_buffer_rsrc((void*)vc, 0, kv_bytes, 0x00020000);

  if (w < NW) {
    const int wv = w * NSPLIT + s;
    const int btv = bt[min(wv + lane * T, nbps - 1)];
    const int len_raw = lens[0];
    u32x4 qv[G];
#pragma unroll
    for (int g = 0; g < G; ++g) qv[g] = q[(head * G + g) * (D / 8) + c];
    const int skv = nbps * PAGE;
    const bool all_masked = len_raw <= 0;
    const int len = all_masked ? skv : min(len_raw, skv);
    const float sc = all_masked ? 0.f : 0.0625f * 1.4426950408889634f;
    const int npages = (len + PAGE - 1) / PAGE;
    const int n = npages > wv ? (npages - wv + T - 1) / T : 0;
    const unsigned voff = h * TOK_BYTES + c * 16;
    const unsigned hoff = head * (D * 2);
    auto page_off = [&](int i) -> unsigned {
      const unsigned o = (unsigned)__builtin_amdgcn_readlane(btv, i & 63) * PAGE_BYTES + hoff;
      return i < n ? o : 0xF0000000u;
    };
    float m = NEG_BIG, l = 0.f, acc[2][8];
#pragma unroll
    for (int k = 0; k < 8; ++k) { acc[0][k] = 0.f; acc[1][k] = 0.f; }
    int tok = wv * PAGE + 2 * ((lane & 15) >> 1) + h;
    Tile K, V;
    {
      const unsigned o0 = page_off(0);
      issue(K, krs, voff, o0);
      issue(V, vrs, voff, o0);
      PIN();
    }
    int i = 0;
    do {
      float x[16];
#pragma unroll
      for (int j = 0; j < 8; ++j)
#pragma unroll
        for (int g = 0; g < G; ++g) {
          float d = 0.f;
#pragma unroll
          for (int k = 0; k < 4; ++k) d = dot2(K.r[j][k], qv[g][k], d);
          x[2 * j + g] = d;
        }
      ++i;
      const unsigned onext = page_off(i);
      PIN();
      issue(K, krs, voff, onext);
      PIN();
#define TR_STEP(HALF, CTRL, BIT)                                            \
  _Pragma("unroll") for (int u = 0; u < (HALF); ++u) {                      \
    const float lo_ = x[u], hi_ = x[u + (HALF)];                            \
    const float t1_ = lo_ + dpp<CTRL>(lo_), t2_ = hi_ + dpp<CTRL>(hi_);     \
    x[u] = (lane & (BIT)) ? t2_ : t1_;                                      \
  }
      TR_STEP(8, ROR8, 8) TR_STEP(4, HMIRROR, 4) TR_STEP(2, XOR2, 2) TR_STEP(1, XOR1, 1)
#undef TR_STEP
      float sv = xor16_sum(x[0]) * sc;
      sv = tok < len ? sv : -INFINITY;
      tok += T * PAGE;
      float pm = fmaxf(sv, dpp<XOR2>(sv));
      pm = fmaxf(pm, dpp<ROR4>(pm));
      pm = fmaxf(pm, dpp<ROR8>(pm));
      const float mn = fmaxf(m, pm);
      if (__builtin_amdgcn_ballot_w64(mn > m)) {
        const float alpha = __builtin_amdgcn_exp2f(m - mn);
        l *= alpha;
        const float a0 = dpp<0x00>(alpha), a1 = dpp<0x55>(alpha);
#pragma unroll
        for (int k = 0; k < 8; ++k) { acc[0][k] *= a0; acc[1][k] *= a1; }
        m = mn;
      }
      const float p = __builtin_amdgcn_exp2f(sv - m);
      l += p;
      pv_step<0>(V.r[0], p, acc); pv_step<1>(V.r[1], p, acc);
      pv_step<2>(V.r[2], p, acc); pv_step<3>(V.r[3], p, acc);
      pv_step<4>(V.r[4], p, acc); pv_step<5>(V.r[5], p, acc);
      pv_step<6>(V.r[6], p, acc); pv_step<7>(V.r[7], p, acc);
      PIN();
      issue(V, vrs, voff, onext);
      PIN();
    } while (i < n);
    l += dpp<XOR2>(l);
    l += dpp<ROR4>(l);
    l += dpp<ROR8>(l);
#pragma unroll
    for (int g = 0; g < G; ++g) {
      float* rec = lds + ((w * 2 + h) * G + g) * REC;
      *reinterpret_cast<f32x4*>(rec + 4 + 8 * c) = f32x4{acc[g][0], acc[g][1], acc[g][2], acc[g][3]};
      *reinterpret_cast<f32x4*>(rec + 8 + 8 * c) = f32x4{acc[g][4], acc[g][5], acc[g][6], acc[g][7]};
    }
    if (c < 2) *reinterpret_cast<f32x2*>(lds + ((w * 2 + h) * G + c) * REC) = f32x2{m, l};
    __syncthreads();
    return;
  }

  const __amdgpu_buffer_rsrc_t wrs = __builtin_amdgcn_make_buffer_rsrc(ws, 0, WS_BYTES, 0x00020000);
  const unsigned slot = WS_P + (head * NSPLIT + s) * (G * D * 4);
  const unsigned put_f = lane == 0 ? WS_F + (head * NSPLIT + s) * 128 : OOB;
  const unsigned get_f = WS_F + (head * NSPLIT + c) * 128;
  const u32x2 e = __builtin_amdgcn_raw_buffer_load_b64(wrs, 0, WS_E + head * 256, AUX_SC1);
  {
    const u32x4 sent4 = {SENT, SENT, SENT, SENT};
    __builtin_amdgcn_raw_buffer_store_b128(sent4, wrs, lane * 16, slot, AUX_SC1);
    __builtin_amdgcn_raw_buffer_store_b128(sent4, wrs, lane * 16 + 1024, slot, AUX_SC1);
    __builtin_amdgcn_raw_buffer_store_b64(u32x2{SENT, SENT}, wrs, put_f + 8, 0, AUX_SC1);
  }
  const unsigned long long enext = (((unsigned long long)e[1] << 32) | e[0]) + 1ull;
  const unsigned fa_lo = (unsigned)(enext << 1), fa_hi = (unsigned)(enext >> 31);
  asm volatile("s_waitcnt vmcnt(0)" ::: "memory");
  __builtin_amdgcn_raw_buffer_store_b64(u32x2{fa_lo, fa_hi}, wrs, put_f, 0, AUX_SC1);
  const int g_out = s >> 4;
  const unsigned get = (c == s) ? OOB : WS_P + ((head * NSPLIT + c) * G + g_out) * (D * 4) + (16 * (s & 15) + 8 * h) * 4;
  const unsigned get_l = get_f + 8 + g_out * 4;
  __syncthreads();
  u32x2 f = __builtin_amdgcn_raw_buffer_load_b64(wrs, get_f, 0, AUX_SC1);
  PIN();
  u32x4 pa, pb;
  unsigned pl;
  {
    f32x2 ml[G][2 * NW];
    f32x4 ra[G][2 * NW];
#pragma unroll
    for (int g = 0; g < G; ++g)
#pragma unroll
      for (int r = 0; r < 2 * NW; ++r) ml[g][r] = *reinterpret_cast<const f32x2*>(lds + (r * G + g) * REC);
#pragma unroll
    for (int g = 0; g < G; ++g)
#pragma unroll
      for (int r = 0; r < 2 * NW; ++r) ra[g][r] = *reinterpret_cast<const f32x4*>(lds + (r * G + g) * REC + 4 + 4 * lane);
    __builtin_amdgcn_sched_barrier(0);
    u32x4 uo[G];
    unsigned ul[G];
#pragma unroll
    for (int g = 0; g < G; ++g) {
      float M = NEG_BIG;
#pragma unroll
      for (int r = 0; r < 2 * NW; ++r) M = fmaxf(M, ml[g][r][0]);
      float L = 0.f;
      f32x4 a = {0.f, 0.f, 0.f, 0.f};
#pragma unroll
      for (int r = 0; r < 2 * NW; ++r) {
        const float wt = __builtin_amdgcn_exp2f(ml[g][r][0] - M);
        L = fmaf(wt, ml[g][r][1], L);
        a += wt * ra[g][r];
      }
      const float inv = L > 0.f ? __builtin_amdgcn_rcpf(L) : 0.f;
      const float lse = L > 0.f ? M + __builtin_amdgcn_logf(L) : NEG_BIG;
      a *= inv;
      uo[g] = u32x4{not_sent(a[0]), not_sent(a[1]), not_sent(a[2]), not_sent(a[3])};
      ul[g] = not_sent(lse);
    }
    __builtin_amdgcn_raw_buffer_store_b128(uo[0], wrs, lane * 16, slot, AUX_SC1);
    __builtin_amdgcn_raw_buffer_store_b128(uo[1], wrs, lane * 16 + 1024, slot, AUX_SC1);
    __builtin_amdgcn_raw_buffer_store_b128(u32x4{fa_lo | 1u, fa_hi, ul[0], ul[1]}, wrs, put_f, 0, AUX_SC1);
    PIN();
    const u32x4 own = g_out ? uo[1] : uo[0];
    const int src = (4 * (s & 15) + 2 * h) * 4;
#pragma unroll
    for (int k = 0; k < 4; ++k) {
      pa[k] = __builtin_amdgcn_ds_bpermute(src, own[k]);
      pb[k] = __builtin_amdgcn_ds_bpermute(src + 4, own[k]);
    }
    pl = g_out ? ul[1] : ul[0];
  }
  {
    auto gate_closed = [&]() {
      const bool mine = (f[0] | 1u) == (fa_lo | 1u) && f[1] == fa_hi;
      const unsigned long long bad = __builtin_amdgcn_ballot_w64(!mine);
      const unsigned long long pub = __builtin_amdgcn_ballot_w64((f[0] & 1u) || c == s);
      return bad != 0 || __builtin_popcountll(pub) < 2 * GATE;
    };
    if (gate_closed()) {
      unsigned spin = 0;
      bool closed;
#pragma nounroll
      do {
        __builtin_amdgcn_s_sleep(GATESLEEP);
        asm volatile("" ::: "memory");
        if (!(f[0] == (fa_lo | 1u) && f[1] == fa_hi)) f = __builtin_amdgcn_raw_buffer_load_b64(wrs, get_f, 0, AUX_SC1);
        closed = gate_closed();
      } while (closed && ++spin < SPIN_CAP);
    }
    asm volatile("" ::: "memory");
    if (s == 0)
      __builtin_amdgcn_raw_buffer_store_b64(u32x2{(unsigned)enext, (unsigned)(enext >> 32)}, wrs, lane == 0 ? WS_E + head * 256 : OOB, 0, AUX_SC1);
    bool pending = c != s;
    unsigned spin = 0;
#pragma nounroll
    do {
      asm volatile("" ::: "memory");
      if (pending) {
        pa = __builtin_amdgcn_raw_buffer_load_b128(wrs, get, 0, AUX_SC1);
        pb = __builtin_amdgcn_raw_buffer_load_b128(wrs, get + 16, 0, AUX_SC1);
        pl = __builtin_amdgcn_raw_buffer_load_b32(wrs, get_l, 0, AUX_SC1);
        pending = max(max(max(pa[0], pa[1]), max(pa[2], pa[3])), max(max(max(pb[0], pb[1]), max(pb[2], pb[3])), pl)) == SENT;
      }
    } while (__builtin_amdgcn_ballot_w64(pending) != 0 && ++spin < SPIN_CAP);
    const float lse = __uint_as_float(pl);
    const float M = max32(lse);
    const float wt = __builtin_amdgcn_exp2f(lse - M);
    const float L = sum32(wt);
    float o[8];
#pragma unroll
    for (int k = 0; k < 4; ++k) {
      o[k] = sum32(wt * __uint_as_float(pa[k]));
      o[4 + k] = sum32(wt * __uint_as_float(pb[k]));
    }
    const float inv = 1.f / L;
    if (c == 0) {
      const u32x4 r = {pack_bf16(o[0] * inv, o[1] * inv), pack_bf16(o[2] * inv, o[3] * inv),
                       pack_bf16(o[4] * inv, o[5] * inv), pack_bf16(o[6] * inv, o[7] * inv)};
      *reinterpret_cast<u32x4*>(out + (head * G * D + 16 * s + 8 * h) * 2) = r;
    }
  }
}

}

extern "C" void kernel_launch(void* const* d_in, const int* in_sizes, int n_in, void* d_out, int out_size,
                              void* d_ws, size_t ws_size, hipStream_t stream) {
  const int nbps = in_sizes[3];
  const unsigned kv_bytes = (unsigned)in_sizes[1] * 2u;
  attn_decode<<<HKV * NSPLIT, (NW + 1) * 64, 0, stream>>>((const u32x4*)d_in[0], d_in[1], d_in[2], (const int*)d_in[3],
                                                          (const int*)d_in[4], (unsigned char*)d_ws,
                                                          (unsigned char*)d_out, nbps, kv_bytes);
}
```

```cpp
#define AMPMODE 0
#include <hip/hip_runtime.h>
#include <hip/hip_bf16.h>
#include <stdint.h>

#define NW 4
#define LBW 2
#define GATE 32
#define GATESLEEP 2
#define TLBWARM 0

namespace {

constexpr int D = 256, PAGE = 16, HKV = 8, G = 2, NSPLIT = 32;
constexpr int T = NW * NSPLIT;
constexpr unsigned PAGE_BYTES = PAGE * HKV * D * 2;
constexpr unsigned TOK_BYTES = HKV * D * 2;
constexpr float NEG_BIG = -3.0e38f;
constexpr int AUX_NT = 2, AUX_SC1 = 16;
constexpr unsigned SENT = 0xFFFFFFFFu;
constexpr unsigned WS_E = 0, WS_F = 4096, WS_P = WS_F + HKV * NSPLIT * 128, WS_BYTES = WS_P + HKV * NSPLIT * G * D * 4;
constexpr unsigned OOB = 0xF0000000u;
constexpr int REC = D + 4;
constexpr unsigned SPIN_CAP = 400000;

typedef unsigned u32x4 __attribute__((ext_vector_type(4)));
typedef unsigned u32x2 __attribute__((ext_vector_type(2)));
typedef float f32x4 __attribute__((ext_vector_type(4)));
typedef float f32x2 __attribute__((ext_vector_type(2)));
typedef __bf16 bf16x2 __attribute__((ext_vector_type(2)));

__device__ __forceinline__ float dot2(unsigned a, unsigned b, float c) {
  return __builtin_amdgcn_fdot2_f32_bf16(__builtin_bit_cast(bf16x2, a), __builtin_bit_cast(bf16x2, b), c, false);
}
template <int CTRL> __device__ __forceinline__ float dpp(float x) {
  return __builtin_bit_cast(float, __builtin_amdgcn_mov_dpp(__builtin_bit_cast(int, x), CTRL, 0xf, 0xf, true));
}
constexpr int XOR1 = 0xB1, XOR2 = 0x4E, HMIRROR = 0x141, ROR8 = 0x128, ROR4 = 0x124, NEWBCAST = 0x150;

__device__ __forceinline__ float xor16_sum(float x) {
  auto s = __builtin_amdgcn_permlane16_swap(__float_as_uint(x), __float_as_uint(x), false, false);
  return __uint_as_float(s[0]) + __uint_as_float(s[1]);
}
__device__ __forceinline__ float xor16_max(float x) {
  auto s = __builtin_amdgcn_permlane16_swap(__float_as_uint(x), __float_as_uint(x), false, false);
  return fmaxf(__uint_as_float(s[0]), __uint_as_float(s[1]));
}
__device__ __forceinline__ float sum32(float x) {
  x += dpp<XOR1>(x); x += dpp<XOR2>(x); x += dpp<HMIRROR>(x); x += dpp<ROR8>(x);
  return xor16_sum(x);
}
__device__ __forceinline__ float max32(float x) {
  x = fmaxf(x, dpp<XOR1>(x)); x = fmaxf(x, dpp<XOR2>(x)); x = fmaxf(x, dpp<HMIRROR>(x)); x = fmaxf(x, dpp<ROR8>(x));
  return xor16_max(x);
}
__device__ __forceinline__ unsigned pack_bf16(float lo, float hi) {
  const bf16x2 v = {(__bf16)lo, (__bf16)hi};
  return __builtin_bit_cast(unsigned, v);
}
__device__ __forceinline__ unsigned not_sent(float x) { return min(__float_as_uint(x), SENT - 1u); }

#define PIN() do { asm volatile("" ::: "memory"); __builtin_amdgcn_sched_barrier(0); } while (0)
struct Tile { u32x4 r[8]; };

__device__ __forceinline__ void issue(Tile& t, __amdgpu_buffer_rsrc_t rs, unsigned voff, unsigned soff) {
#pragma unroll
  for (int j = 0; j < 8; ++j) {
    t.r[j] = __builtin_amdgcn_raw_buffer_load_b128(rs, voff, soff + j * 2 * TOK_BYTES, AUX_NT);
    __builtin_amdgcn_sched_barrier(0);
  }
}

template <int J> __device__ __forceinline__ void pv_step(const u32x4& v, float p, float (&acc)[2][8]) {
  const float p0 = dpp<NEWBCAST + 2 * J>(p), p1 = dpp<NEWBCAST + 2 * J + 1>(p);
#pragma unroll
  for (int k = 0; k < 4; ++k) {
    const float lo = __uint_as_float(v[k] << 16), hi = __uint_as_float(v[k] & 0xffff0000u);
    acc[0][2 * k] = fmaf(p0, lo, acc[0][2 * k]);
    acc[0][2 * k + 1] = fmaf(p0, hi, acc[0][2 * k + 1]);
    acc[1][2 * k] = fmaf(p1, lo, acc[1][2 * k]);
    acc[1][2 * k + 1] = fmaf(p1, hi, acc[1][2 * k + 1]);
  }
  __builtin_amdgcn_sched_barrier(0);
}

__global__ void __launch_bounds__((NW + 1) * 64, LBW)
attn_decode(const u32x4* __restrict__ q, const void* __restrict__ kc, const void* __restrict__ vc,
            const int* __restrict__ bt, const int* __restrict__ lens, unsigned char* __restrict__ ws,
            unsigned char* __restrict__ out, int nbps, unsigned kv_bytes) {
  __shared__ __attribute__((aligned(16))) float lds[NW * 2 * G * REC];
  unsigned long long t_start = __builtin_amdgcn_s_memrealtime();
  __shared__ unsigned long long t_sh[2];
  asm volatile("" ::"s"(q), "s"(kc), "s"(vc), "s"(bt), "s"(lens), "s"(ws), "s"(out), "s"(nbps), "s"(kv_bytes));
  const int head = blockIdx.x & (HKV - 1), s = blockIdx.x >> 3;
  const int lane = threadIdx.x & 63;
  const int w = __builtin_amdgcn_readfirstlane(threadIdx.x >> 6);
  const int h = lane >> 5, c = lane & 31;
  const __amdgpu_buffer_rsrc_t krs = __builtin_amdgcn_make_buffer_rsrc((void*)kc, 0, kv_bytes, 0x00020000);
  const __amdgpu_buffer_rsrc_t vrs = __builtin_amdgcn_make_buffer_rsrc((void*)vc, 0, kv_bytes, 0x00020000);

  if (w < NW) {
    const int wv = w * NSPLIT + s;
    const int btv = bt[min(wv + lane * T, nbps - 1)];
    const int len_raw = lens[0];
    u32x4 qv[G];
#pragma unroll
    for (int g = 0; g < G; ++g) qv[g] = q[(head * G + g) * (D / 8) + c];
    const int skv = nbps * PAGE;
    const bool all_masked = len_raw <= 0;
    const int len = all_masked ? skv : min(len_raw, skv);
    const float sc = all_masked ? 0.f : 0.0625f * 1.4426950408889634f;
    const int npages = (len + PAGE - 1) / PAGE;
    const int n = npages > wv ? (npages - wv + T - 1) / T : 0;
    const unsigned voff = h * TOK_BYTES + c * 16;
    const unsigned hoff = head * (D * 2);
    auto page_off = [&](int i) -> unsigned {
      const unsigned o = (unsigned)__builtin_amdgcn_readlane(btv, i & 63) * PAGE_BYTES + hoff;
      return i < n ? o : 0xF0000000u;
    };
    float m = NEG_BIG, l = 0.f, acc[2][8];
#pragma unroll
    for (int k = 0; k < 8; ++k) { acc[0][k] = 0.f; acc[1][k] = 0.f; }
    int tok = wv * PAGE + 2 * ((lane & 15) >> 1) + h;
    Tile K, V;
    {
      const unsigned o0 = page_off(0);
      issue(K, krs, voff, o0);
      issue(V, vrs, voff, o0);
      PIN();
      if (w == 0 && lane == 0) t_sh[0] = __builtin_amdgcn_s_memrealtime() - t_start;
    }
    int i = 0;
    do {
      float x[16];
#pragma unroll
      for (int j = 0; j < 8; ++j)
#pragma unroll
        for (int g = 0; g < G; ++g) {
          float d = 0.f;
#pragma unroll
          for (int k = 0; k < 4; ++k) d = dot2(K.r[j][k], qv[g][k], d);
          x[2 * j + g] = d;
        }
      ++i;
      const unsigned onext = page_off(i);
      PIN();
      issue(K, krs, voff, onext);
      PIN();
#define TR_STEP(HALF, CTRL, BIT)                                            \
  _Pragma("unroll") for (int u = 0; u < (HALF); ++u) {                      \
    const float lo_ = x[u], hi_ = x[u + (HALF)];                            \
    const float t1_ = lo_ + dpp<CTRL>(lo_), t2_ = hi_ + dpp<CTRL>(hi_);     \
    x[u] = (lane & (BIT)) ? t2_ : t1_;                                      \
  }
      TR_STEP(8, ROR8, 8) TR_STEP(4, HMIRROR, 4) TR_STEP(2, XOR2, 2) TR_STEP(1, XOR1, 1)
#undef TR_STEP
      float sv = xor16_sum(x[0]) * sc;
      sv = tok < len ? sv : -INFINITY;
      tok += T * PAGE;
      float pm = fmaxf(sv, dpp<XOR2>(sv));
      pm = fmaxf(pm, dpp<ROR4>(pm));
      pm = fmaxf(pm, dpp<ROR8>(pm));
      const float mn = fmaxf(m, pm);
      if (__builtin_amdgcn_ballot_w64(mn > m)) {
        const float alpha = __builtin_amdgcn_exp2f(m - mn);
        l *= alpha;
        const float a0 = dpp<0x00>(alpha), a1 = dpp<0x55>(alpha);
#pragma unroll
        for (int k = 0; k < 8; ++k) { acc[0][k] *= a0; acc[1][k] *= a1; }
        m = mn;
      }
      const float p = __builtin_amdgcn_exp2f(sv - m);
      l += p;
      pv_step<0>(V.r[0], p, acc); pv_step<1>(V.r[1], p, acc);
      pv_step<2>(V.r[2], p, acc); pv_step<3>(V.r[3], p, acc);
      pv_step<4>(V.r[4], p, acc); pv_step<5>(V.r[5], p, acc);
      pv_step<6>(V.r[6], p, acc); pv_step<7>(V.r[7], p, acc);
      PIN();
      issue(V, vrs, voff, onext);
      PIN();
    } while (i < n);
    l += dpp<XOR2>(l);
    l += dpp<ROR4>(l);
    l += dpp<ROR8>(l);
#pragma unroll
    for (int g = 0; g < G; ++g) {
      float* rec = lds + ((w * 2 + h) * G + g) * REC;
      *reinterpret_cast<f32x4*>(rec + 4 + 8 * c) = f32x4{acc[g][0], acc[g][1], acc[g][2], acc[g][3]};
      *reinterpret_cast<f32x4*>(rec + 8 + 8 * c) = f32x4{acc[g][4], acc[g][5], acc[g][6], acc[g][7]};
    }
    if (c < 2) *reinterpret_cast<f32x2*>(lds + ((w * 2 + h) * G + c) * REC) = f32x2{m, l};
    __syncthreads();
    return;
  }

  const __amdgpu_buffer_rsrc_t wrs = __builtin_amdgcn_make_buffer_rsrc(ws, 0, WS_BYTES, 0x00020000);
  const unsigned slot = WS_P + (head * NSPLIT + s) * (G * D * 4);
  const unsigned put_f = lane == 0 ? WS_F + (head * NSPLIT + s) * 128 : OOB;
  const unsigned get_f = WS_F + (head * NSPLIT + c) * 128;
  const u32x2 e = __builtin_amdgcn_raw_buffer_load_b64(wrs, 0, WS_E + head * 256, AUX_SC1);
  {
    const u32x4 sent4 = {SENT, SENT, SENT, SENT};
    __builtin_amdgcn_raw_buffer_store_b128(sent4, wrs, lane * 16, slot, AUX_SC1);
    __builtin_amdgcn_raw_buffer_store_b128(sent4, wrs, lane * 16 + 1024, slot, AUX_SC1);
    __builtin_amdgcn_raw_buffer_store_b64(u32x2{SENT, SENT}, wrs, put_f + 8, 0, AUX_SC1);
  }
  const unsigned long long enext = (((unsigned long long)e[1] << 32) | e[0]) + 1ull;
  const unsigned fa_lo = (unsigned)(enext << 1), fa_hi = (unsigned)(enext >> 31);
  asm volatile("s_waitcnt vmcnt(0)" ::: "memory");
  __builtin_amdgcn_raw_buffer_store_b64(u32x2{fa_lo, fa_hi}, wrs, put_f, 0, AUX_SC1);
  const int g_out = s >> 4;
  const unsigned get = (c == s) ? OOB : WS_P + ((head * NSPLIT + c) * G + g_out) * (D * 4) + (16 * (s & 15) + 8 * h) * 4;
  const unsigned get_l = get_f + 8 + g_out * 4;
  __syncthreads();
  unsigned long long t_bar = __builtin_amdgcn_s_memrealtime();
  u32x2 f = __builtin_amdgcn_raw_buffer_load_b64(wrs, get_f, 0, AUX_SC1);
  PIN();
  u32x4 pa, pb;
  unsigned pl;
  {
    f32x2 ml[G][2 * NW];
    f32x4 ra[G][2 * NW];
#pragma unroll
    for (int g = 0; g < G; ++g)
#pragma unroll
      for (int r = 0; r < 2 * NW; ++r) ml[g][r] = *reinterpret_cast<const f32x2*>(lds + (r * G + g) * REC);
#pragma unroll
    for (int g = 0; g < G; ++g)
#pragma unroll
      for (int r = 0; r < 2 * NW; ++r) ra[g][r] = *reinterpret_cast<const f32x4*>(lds + (r * G + g) * REC + 4 + 4 * lane);
    __builtin_amdgcn_sched_barrier(0);
    u32x4 uo[G];
    unsigned ul[G];
#pragma unroll
    for (int g = 0; g < G; ++g) {
      float M = NEG_BIG;
#pragma unroll
      for (int r = 0; r < 2 * NW; ++r) M = fmaxf(M, ml[g][r][0]);
      float L = 0.f;
      f32x4 a = {0.f, 0.f, 0.f, 0.f};
#pragma unroll
      for (int r = 0; r < 2 * NW; ++r) {
        const float wt = __builtin_amdgcn_exp2f(ml[g][r][0] - M);
        L = fmaf(wt, ml[g][r][1], L);
        a += wt * ra[g][r];
      }
      const float inv = L > 0.f ? __builtin_amdgcn_rcpf(L) : 0.f;
      const float lse = L > 0.f ? M + __builtin_amdgcn_logf(L) : NEG_BIG;
      a *= inv;
      uo[g] = u32x4{not_sent(a[0]), not_sent(a[1]), not_sent(a[2]), not_sent(a[3])};
      ul[g] = not_sent(lse);
    }
    __builtin_amdgcn_raw_buffer_store_b128(uo[0], wrs, lane * 16, slot, AUX_SC1);
    __builtin_amdgcn_raw_buffer_store_b128(uo[1], wrs, lane * 16 + 1024, slot, AUX_SC1);
    __builtin_amdgcn_raw_buffer_store_b128(u32x4{fa_lo | 1u, fa_hi, ul[0], ul[1]}, wrs, put_f, 0, AUX_SC1);
    PIN();
    const u32x4 own = g_out ? uo[1] : uo[0];
    const int src = (4 * (s & 15) + 2 * h) * 4;
#pragma unroll
    for (int k = 0; k < 4; ++k) {
      pa[k] = __builtin_amdgcn_ds_bpermute(src, own[k]);
      pb[k] = __builtin_amdgcn_ds_bpermute(src + 4, own[k]);
    }
    pl = g_out ? ul[1] : ul[0];
  }
  {
    auto gate_closed = [&]() {
      const bool mine = (f[0] | 1u) == (fa_lo | 1u) && f[1] == fa_hi;
      const unsigned long long bad = __builtin_amdgcn_ballot_w64(!mine);
      const unsigned long long pub = __builtin_amdgcn_ballot_w64((f[0] & 1u) || c == s);
      return bad != 0 || __builtin_popcountll(pub) < 2 * GATE;
    };
    if (gate_closed()) {
      unsigned spin = 0;
      bool closed;
#pragma nounroll
      do {
        __builtin_amdgcn_s_sleep(GATESLEEP);
        asm volatile("" ::: "memory");
        if (!(f[0] == (fa_lo | 1u) && f[1] == fa_hi)) f = __builtin_amdgcn_raw_buffer_load_b64(wrs, get_f, 0, AUX_SC1);
        closed = gate_closed();
      } while (closed && ++spin < SPIN_CAP);
    }
    asm volatile("" ::: "memory");
    if (s == 0)
      __builtin_amdgcn_raw_buffer_store_b64(u32x2{(unsigned)enext, (unsigned)(enext >> 32)}, wrs, lane == 0 ? WS_E + head * 256 : OOB, 0, AUX_SC1);
    bool pending = c != s;
    unsigned spin = 0;
#pragma nounroll
    do {
      asm volatile("" ::: "memory");
      if (pending) {
        pa = __builtin_amdgcn_raw_buffer_load_b128(wrs, get, 0, AUX_SC1);
        pb = __builtin_amdgcn_raw_buffer_load_b128(wrs, get + 16, 0, AUX_SC1);
        pl = __builtin_amdgcn_raw_buffer_load_b32(wrs, get_l, 0, AUX_SC1);
        pending = max(max(max(pa[0], pa[1]), max(pa[2], pa[3])), max(max(max(pb[0], pb[1]), max(pb[2], pb[3])), pl)) == SENT;
      }
    } while (__builtin_amdgcn_ballot_w64(pending) != 0 && ++spin < SPIN_CAP);
    const float lse = __uint_as_float(pl);
    const float M = max32(lse);
    const float wt = __builtin_amdgcn_exp2f(lse - M);
    const float L = sum32(wt);
    float o[8];
#pragma unroll
    for (int k = 0; k < 4; ++k) {
      o[k] = sum32(wt * __uint_as_float(pa[k]));
      o[4 + k] = sum32(wt * __uint_as_float(pb[k]));
    }
    const float inv = 1.f / L;
    if (c == 0) {
      const u32x4 r = {pack_bf16(o[0] * inv, o[1] * inv), pack_bf16(o[2] * inv, o[3] * inv),
                       pack_bf16(o[4] * inv, o[5] * inv), pack_bf16(o[6] * inv, o[7] * inv)};
      *reinterpret_cast<u32x4*>(out + (head * G * D + 16 * s + 8 * h) * 2) = r;
    }
  }
  {
    const unsigned long long t_end = __builtin_amdgcn_s_memrealtime();
    const unsigned long long extra = 0;
    while (__builtin_amdgcn_s_memrealtime() - t_end < extra) __builtin_amdgcn_s_sleep(4);
  }
}

}

extern "C" void kernel_launch(void* const* d_in, const int* in_sizes, int n_in, void* d_out, int out_size,
                              void* d_ws, size_t ws_size, hipStream_t stream) {
  const int nbps = in_sizes[3];
  const unsigned kv_bytes = (unsigned)in_sizes[1] * 2u;
  attn_decode<<<HKV * NSPLIT, (NW + 1) * 64, 0, stream>>>((const u32x4*)d_in[0], d_in[1], d_in[2], (const int*)d_in[3],
                                                          (const int*)d_in[4], (unsigned char*)d_ws,
                                                          (unsigned char*)d_out, nbps, kv_bytes);
}
```

```cpp
#include <hip/hip_runtime.h>
#include <hip/hip_bf16.h>
#include <stdint.h>

#define NW 4
#define GATESLEEP 2
#define DRYLOOP 0
#define DRYTAIL 1
#define WARM 0
#define SFIRST 1

namespace {

constexpr int D = 256, PAGE = 16, HKV = 8, G = 2, NSPLIT = 32;
constexpr int T = NW * NSPLIT;
constexpr unsigned PAGE_BYTES = PAGE * HKV * D * 2;
constexpr unsigned TOK_BYTES = HKV * D * 2;
constexpr float NEG_BIG = -3.0e38f;
constexpr int AUX_NT = 2, AUX_SC1 = 16;
constexpr unsigned SENT = 0xFFFFFFFFu;
constexpr unsigned WS_E = 0, WS_F = 4096, WS_P = WS_F + HKV * NSPLIT * 128, WS_BYTES = WS_P + HKV * NSPLIT * G * D * 4;
constexpr unsigned OOB = 0xF0000000u;
constexpr int REC = D + 4;
constexpr unsigned SPIN_CAP = 400000;

typedef unsigned u32x4 __attribute__((ext_vector_type(4)));
typedef unsigned u32x2 __attribute__((ext_vector_type(2)));
typedef float f32x4 __attribute__((ext_vector_type(4)));
typedef float f32x2 __attribute__((ext_vector_type(2)));
typedef __bf16 bf16x2 __attribute__((ext_vector_type(2)));
typedef const __attribute__((address_space(4))) int* cint_p;

__device__ __forceinline__ float dot2(unsigned a, unsigned b, float c) {
  return __builtin_amdgcn_fdot2_f32_bf16(__builtin_bit_cast(bf16x2, a), __builtin_bit_cast(bf16x2, b), c, false);
}
template <int CTRL> __device__ __forceinline__ float dpp(float x) {
  return __builtin_bit_cast(float, __builtin_amdgcn_mov_dpp(__builtin_bit_cast(int, x), CTRL, 0xf, 0xf, true));
}
constexpr int XOR1 = 0xB1, XOR2 = 0x4E, HMIRROR = 0x141, ROR8 = 0x128, ROR4 = 0x124, NEWBCAST = 0x150;

__device__ __forceinline__ float xor16_sum(float x) {
  auto s = __builtin_amdgcn_permlane16_swap(__float_as_uint(x), __float_as_uint(x), false, false);
  return __uint_as_float(s[0]) + __uint_as_float(s[1]);
}
__device__ __forceinline__ float xor16_max(float x) {
  auto s = __builtin_amdgcn_permlane16_swap(__float_as_uint(x), __float_as_uint(x), false, false);
  return fmaxf(__uint_as_float(s[0]), __uint_as_float(s[1]));
}
__device__ __forceinline__ float sum32(float x) {
  x += dpp<XOR1>(x); x += dpp<XOR2>(x); x += dpp<HMIRROR>(x); x += dpp<ROR8>(x);
  return xor16_sum(x);
}
__device__ __forceinline__ float max32(float x) {
  x = fmaxf(x, dpp<XOR1>(x)); x = fmaxf(x, dpp<XOR2>(x)); x = fmaxf(x, dpp<HMIRROR>(x)); x = fmaxf(x, dpp<ROR8>(x));
  return xor16_max(x);
}
__device__ __forceinline__ unsigned pack_bf16(float lo, float hi) {
  const bf16x2 v = {(__bf16)lo, (__bf16)hi};
  return __builtin_bit_cast(unsigned, v);
}
__device__ __forceinline__ unsigned not_sent(float x) { return min(__float_as_uint(x), SENT - 1u); }

#define PIN() do { asm volatile("" ::: "memory"); __builtin_amdgcn_sched_barrier(0); } while (0)
struct Tile { u32x4 r[8]; };

__device__ __forceinline__ void issue(Tile& t, __amdgpu_buffer_rsrc_t rs, unsigned voff, unsigned soff) {
#pragma unroll
  for (int j = 0; j < 8; ++j) {
    t.r[j] = __builtin_amdgcn_raw_buffer_load_b128(rs, voff, soff + j * 2 * TOK_BYTES, AUX_NT);
    __builtin_amdgcn_sched_barrier(0);
  }
}

template <int J> __device__ __forceinline__ void pv_step(const u32x4& v, float p, float (&acc)[2][8]) {
  const float p0 = dpp<NEWBCAST + 2 * J>(p), p1 = dpp<NEWBCAST + 2 * J + 1>(p);
#pragma unroll
  for (int k = 0; k < 4; ++k) {
    const float lo = __uint_as_float(v[k] << 16), hi = __uint_as_float(v[k] & 0xffff0000u);
    acc[0][2 * k] = fmaf(p0, lo, acc[0][2 * k]);
    acc[0][2 * k + 1] = fmaf(p0, hi, acc[0][2 * k + 1]);
    acc[1][2 * k] = fmaf(p1, lo, acc[1][2 * k]);
    acc[1][2 * k + 1] = fmaf(p1, hi, acc[1][2 * k + 1]);
  }
  __builtin_amdgcn_sched_barrier(0);
}

__global__ void __launch_bounds__((NW + 1) * 64, 2)
attn_decode(const u32x4* __restrict__ q, const void* __restrict__ kc, const void* __restrict__ vc,
            const int* __restrict__ bt, const int* __restrict__ lens, unsigned char* __restrict__ ws,
            unsigned char* __restrict__ out, int nbps, unsigned kv_bytes) {
  __shared__ __attribute__((aligned(16))) float lds[(NW + 1) * 2 * G * REC];
  asm volatile("" ::"s"(q), "s"(kc), "s"(vc), "s"(bt), "s"(lens), "s"(ws), "s"(out), "s"(nbps), "s"(kv_bytes));
  const int head = blockIdx.x & (HKV - 1), s = blockIdx.x >> 3;
  const int lane = threadIdx.x & 63;
  const int w = __builtin_amdgcn_readfirstlane(threadIdx.x >> 6);
  const int h = lane >> 5, c = lane & 31;
  const bool helper = w == NW;

  if (DRYLOOP || !helper) {
    const __amdgpu_buffer_rsrc_t krs = __builtin_amdgcn_make_buffer_rsrc((void*)kc, 0, kv_bytes, 0x00020000);
    const __amdgpu_buffer_rsrc_t vrs = __builtin_amdgcn_make_buffer_rsrc((void*)vc, 0, kv_bytes, 0x00020000);
    const int wv = w * NSPLIT + s;
    const int len_raw = ((cint_p)lens)[0];
    const int bt0 = ((cint_p)bt)[min(wv, nbps - 1)];
    const int btv = bt[min(wv + lane * T, nbps - 1)];
    u32x4 qv[G];
#pragma unroll
    for (int g = 0; g < G; ++g) qv[g] = q[(head * G + g) * (D / 8) + c];
    __builtin_amdgcn_sched_barrier(0);
    const int skv = nbps * PAGE;
    const bool all_masked = len_raw <= 0;
    const int len = all_masked ? skv : min(len_raw, skv);
    const float sc = all_masked ? 0.f : 0.0625f * 1.4426950408889634f;
    const int npages = (len + PAGE - 1) / PAGE;
    const int n = (npages > wv && !helper) ? (npages - wv + T - 1) / T : 0;
    const unsigned voff = h * TOK_BYTES + c * 16;
    const unsigned hoff = head * (D * 2);
    auto page_off = [&](int i) -> unsigned {
      const unsigned o = (unsigned)__builtin_amdgcn_readlane(btv, i & 63) * PAGE_BYTES + hoff;
      return i < n ? o : OOB;
    };
    float m = NEG_BIG, l = 0.f, acc[2][8];
#pragma unroll
    for (int k = 0; k < 8; ++k) { acc[0][k] = 0.f; acc[1][k] = 0.f; }
    int tok = helper ? 0x7fff0000 : wv * PAGE + 2 * ((lane & 15) >> 1) + h;
    Tile K, V;
    {
      const unsigned o0 = n > 0 ? (unsigned)bt0 * PAGE_BYTES + hoff : OOB;
      issue(K, krs, voff, o0);
      issue(V, vrs, voff, o0);
      PIN();
    }
    int i = 0;
    do {
      float x[16];
#pragma unroll
      for (int j = 0; j < 8; ++j)
#pragma unroll
        for (int g = 0; g < G; ++g) {
          float d = 0.f;
#pragma unroll
          for (int k = 0; k < 4; ++k) d = dot2(K.r[j][k], qv[g][k], d);
          x[2 * j + g] = d;
        }
      ++i;
      const unsigned onext = page_off(i);
      PIN();
      issue(K, krs, voff, onext);
      PIN();
#define TR_STEP(HALF, CTRL, BIT)                                            \
  _Pragma("unroll") for (int u = 0; u < (HALF); ++u) {                      \
    const float lo_ = x[u], hi_ = x[u + (HALF)];                            \
    const float t1_ = lo_ + dpp<CTRL>(lo_), t2_ = hi_ + dpp<CTRL>(hi_);     \
    x[u] = (lane & (BIT)) ? t2_ : t1_;                                      \
  }
      TR_STEP(8, ROR8, 8) TR_STEP(4, HMIRROR, 4) TR_STEP(2, XOR2, 2) TR_STEP(1, XOR1, 1)
#undef TR_STEP
      float sv = xor16_sum(x[0]) * sc;
      sv = tok < len ? sv : -INFINITY;
      tok += T * PAGE;
      float pm = fmaxf(sv, dpp<XOR2>(sv));
      pm = fmaxf(pm, dpp<ROR4>(pm));
      pm = fmaxf(pm, dpp<ROR8>(pm));
      const float mn = fmaxf(m, pm);
      if (__builtin_amdgcn_ballot_w64(mn > m) || helper) {
        const float alpha = __builtin_amdgcn_exp2f(m - mn);
        l *= alpha;
        const float a0 = dpp<0x00>(alpha), a1 = dpp<0x55>(alpha);
#pragma unroll
        for (int k = 0; k < 8; ++k) { acc[0][k] *= a0; acc[1][k] *= a1; }
        m = mn;
      }
      const float p = __builtin_amdgcn_exp2f(sv - m);
      l += p;
      pv_step<0>(V.r[0], p, acc); pv_step<1>(V.r[1], p, acc);
      pv_step<2>(V.r[2], p, acc); pv_step<3>(V.r[3], p, acc);
      pv_step<4>(V.r[4], p, acc); pv_step<5>(V.r[5], p, acc);
      pv_step<6>(V.r[6], p, acc); pv_step<7>(V.r[7], p, acc);
      PIN();
      issue(V, vrs, voff, onext);
      PIN();
    } while (i < n);
    l += dpp<XOR2>(l);
    l += dpp<ROR4>(l);
    l += dpp<ROR8>(l);
#pragma unroll
    for (int g = 0; g < G; ++g) {
      float* rec = lds + ((w * 2 + h) * G + g) * REC;
      *reinterpret_cast<f32x4*>(rec + 4 + 8 * c) = f32x4{acc[g][0], acc[g][1], acc[g][2], acc[g][3]};
      *reinterpret_cast<f32x4*>(rec + 8 + 8 * c) = f32x4{acc[g][4], acc[g][5], acc[g][6], acc[g][7]};
    }
    if (c < 2) *reinterpret_cast<f32x2*>(lds + ((w * 2 + h) * G + c) * REC) = f32x2{m, l};
    if (!helper) {
      __syncthreads();
      return;
    }
  }

  const __amdgpu_buffer_rsrc_t wrs = __builtin_amdgcn_make_buffer_rsrc(ws, 0, WS_BYTES, 0x00020000);
  const __amdgpu_buffer_rsrc_t ors = __builtin_amdgcn_make_buffer_rsrc(out, 0, HKV * G * D * 2, 0x00020000);
  const unsigned slot = WS_P + (head * NSPLIT + s) * (G * D * 4);
  const unsigned put_f = lane == 0 ? WS_F + (head * NSPLIT + s) * 128 : OOB;
  const unsigned get_f = WS_F + (head * NSPLIT + c) * 128;
  const u32x2 e = __builtin_amdgcn_raw_buffer_load_b64(wrs, 0, WS_E + head * 256, AUX_SC1);
  {
    const u32x4 sent4 = {SENT, SENT, SENT, SENT};
    __builtin_amdgcn_raw_buffer_store_b128(sent4, wrs, lane * 16, slot, AUX_SC1);
    __builtin_amdgcn_raw_buffer_store_b128(sent4, wrs, lane * 16 + 1024, slot, AUX_SC1);
    __builtin_amdgcn_raw_buffer_store_b64(u32x2{SENT, SENT}, wrs, put_f + 8, 0, AUX_SC1);
  }
  const unsigned long long enext = (((unsigned long long)e[1] << 32) | e[0]) + 1ull;
  const unsigned fa_lo = (unsigned)(enext << 1), fa_hi = (unsigned)(enext >> 31);
  asm volatile("s_waitcnt vmcnt(0)" ::: "memory");
  __builtin_amdgcn_raw_buffer_store_b64(u32x2{fa_lo, fa_hi}, wrs, put_f, 0, AUX_SC1);
  const int g_out = s >> 4;
  const unsigned get = (c == s) ? OOB : WS_P + ((head * NSPLIT + c) * G + g_out) * (D * 4) + (16 * (s & 15) + 8 * h) * 4;
  const unsigned get_l = get_f + 8 + g_out * 4;
#pragma nounroll
  for (int pass = DRYTAIL ? 0 : 1; pass < 2; ++pass) {
    const unsigned dry = pass ? 0u : 0x08000000u;
    if (pass) __syncthreads();
    u32x2 f = __builtin_amdgcn_raw_buffer_load_b64(wrs, get_f, dry, AUX_SC1);
    PIN();
    u32x4 pa, pb;
    unsigned pl;
    {
      f32x2 ml[G][2 * NW];
      f32x4 ra[G][2 * NW];
#pragma unroll
      for (int g = 0; g < G; ++g)
#pragma unroll
        for (int r = 0; r < 2 * NW; ++r) ml[g][r] = *reinterpret_cast<const f32x2*>(lds + (r * G + g) * REC);
#pragma unroll
      for (int g = 0; g < G; ++g)
#pragma unroll
        for (int r = 0; r < 2 * NW; ++r) ra[g][r] = *reinterpret_cast<const f32x4*>(lds + (r * G + g) * REC + 4 + 4 * lane);
      __builtin_amdgcn_sched_barrier(0);
      u32x4 uo[G];
      unsigned ul[G];
#pragma unroll
      for (int g = 0; g < G; ++g) {
        float M = NEG_BIG;
#pragma unroll
        for (int r = 0; r < 2 * NW; ++r) M = fmaxf(M, ml[g][r][0]);
        float L = 0.f;
        f32x4 a = {0.f, 0.f, 0.f, 0.f};
#pragma unroll
        for (int r = 0; r < 2 * NW; ++r) {
          const float wt = __builtin_amdgcn_exp2f(ml[g][r][0] - M);
          L = fmaf(wt, ml[g][r][1], L);
          a += wt * ra[g][r];
        }
        const float inv = L > 0.f ? __builtin_amdgcn_rcpf(L) : 0.f;
        const float lse = L > 0.f ? M + __builtin_amdgcn_logf(L) : NEG_BIG;
        a *= inv;
        uo[g] = u32x4{not_sent(a[0]), not_sent(a[1]), not_sent(a[2]), not_sent(a[3])};
        ul[g] = not_sent(lse);
      }
      __builtin_amdgcn_raw_buffer_store_b128(uo[0], wrs, lane * 16, slot + dry, AUX_SC1);
      __builtin_amdgcn_raw_buffer_store_b128(uo[1], wrs, lane * 16 + 1024, slot + dry, AUX_SC1);
      __builtin_amdgcn_raw_buffer_store_b128(u32x4{fa_lo | 1u, fa_hi, ul[0], ul[1]}, wrs, put_f, dry, AUX_SC1);
      PIN();
      const u32x4 own = g_out ? uo[1] : uo[0];
      const int src = (4 * (s & 15) + 2 * h) * 4;
#pragma unroll
      for (int k = 0; k < 4; ++k) {
        pa[k] = __builtin_amdgcn_ds_bpermute(src, own[k]);
        pb[k] = __builtin_amdgcn_ds_bpermute(src + 4, own[k]);
      }
      pl = g_out ? ul[1] : ul[0];
    }
    const unsigned cap = pass ? SPIN_CAP : 1u;
    bool wait_f = !(f[0] == (fa_lo | 1u) && f[1] == fa_hi) && c != s;
    if (__builtin_amdgcn_ballot_w64(wait_f) != 0) {
      unsigned spin = 0;
#pragma nounroll
      do {
        __builtin_amdgcn_s_sleep(GATESLEEP);
        asm volatile("" ::: "memory");
        if (wait_f) {
          f = __builtin_amdgcn_raw_buffer_load_b64(wrs, get_f, dry, AUX_SC1);
          wait_f = !(f[0] == (fa_lo | 1u) && f[1] == fa_hi);
        }
      } while (__builtin_amdgcn_ballot_w64(wait_f) != 0 && ++spin < cap);
    }
    asm volatile("" ::: "memory");
    __builtin_amdgcn_raw_buffer_store_b64(u32x2{(unsigned)enext, (unsigned)(enext >> 32)}, wrs,
                                          (lane == 0 && s == 0) ? WS_E + head * 256 : OOB, dry, AUX_SC1);
    bool pending = c != s;
    unsigned spin = 0;
#pragma nounroll
    do {
      asm volatile("" ::: "memory");
      if (pending) {
        pa = __builtin_amdgcn_raw_buffer_load_b128(wrs, get, dry, AUX_SC1);
        pb = __builtin_amdgcn_raw_buffer_load_b128(wrs, get + 16, dry, AUX_SC1);
        pl = __builtin_amdgcn_raw_buffer_load_b32(wrs, get_l, dry, AUX_SC1);
        pending = max(max(max(pa[0], pa[1]), max(pa[2], pa[3])), max(max(max(pb[0], pb[1]), max(pb[2], pb[3])), pl)) == SENT;
      }
    } while (__builtin_amdgcn_ballot_w64(pending) != 0 && ++spin < cap);
    const float lse = __uint_as_float(pl);
    const float M = max32(lse);
    const float wt = __builtin_amdgcn_exp2f(lse - M);
    const float L = sum32(wt);
    float o[8];
#pragma unroll
    for (int k = 0; k < 4; ++k) {
      o[k] = sum32(wt * __uint_as_float(pa[k]));
      o[4 + k] = sum32(wt * __uint_as_float(pb[k]));
    }
    const float inv = __builtin_amdgcn_rcpf(L);
    const u32x4 r = {pack_bf16(o[0] * inv, o[1] * inv), pack_bf16(o[2] * inv, o[3] * inv),
                     pack_bf16(o[4] * inv, o[5] * inv), pack_bf16(o[6] * inv, o[7] * inv)};
    __builtin_amdgcn_raw_buffer_store_b128(r, ors, c == 0 ? (head * G * D + 16 * s + 8 * h) * 2 : OOB, dry, 0);
  }
}

}

extern "C" void kernel_launch(void* const* d_in, const int* in_sizes, int n_in, void* d_out, int out_size,
                              void* d_ws, size_t ws_size, hipStream_t stream) {
  const int nbps = in_sizes[3];
  const unsigned kv_bytes = (unsigned)in_sizes[1] * 2u;
  attn_decode<<<HKV * NSPLIT, (NW + 1) * 64, 0, stream>>>((const u32x4*)d_in[0], d_in[1], d_in[2], (const int*)d_in[3],
                                                          (const int*)d_in[4], (unsigned char*)d_ws,
                                                          (unsigned char*)d_out, nbps, kv_bytes);
}
```

```cpp
#include <hip/hip_runtime.h>
#include <hip/hip_bf16.h>
#include <stdint.h>

#define GATESLEEP 2
#define CODEPF 5120
#define WARM 0
#define HPERM 0x10765432
#define SPEC0 1

namespace {

constexpr int D = 256, PAGE = 16, HKV = 8, G = 2, NSPLIT = 32, NW = 4;
constexpr int T = NW * NSPLIT;
constexpr unsigned PAGE_BYTES = PAGE * HKV * D * 2;
constexpr unsigned TOK_BYTES = HKV * D * 2;
constexpr float NEG_BIG = -3.0e38f;
constexpr int AUX_NT = 2, AUX_SC1 = 16;
constexpr unsigned SENT = 0xFFFFFFFFu;
constexpr unsigned WS_E = 0, WS_F = 4096, WS_P = WS_F + HKV * NSPLIT * 128, WS_BYTES = WS_P + HKV * NSPLIT * G * D * 4;
constexpr unsigned OOB = 0xF0000000u;
constexpr int REC = D + 4;
constexpr unsigned SPIN_CAP = 400000;

typedef unsigned u32x4 __attribute__((ext_vector_type(4)));
typedef unsigned u32x2 __attribute__((ext_vector_type(2)));
typedef float f32x4 __attribute__((ext_vector_type(4)));
typedef float f32x2 __attribute__((ext_vector_type(2)));
typedef __bf16 bf16x2 __attribute__((ext_vector_type(2)));
typedef const __attribute__((address_space(4))) int* cint_p;

__device__ __forceinline__ float dot2(unsigned a, unsigned b, float c) {
  return __builtin_amdgcn_fdot2_f32_bf16(__builtin_bit_cast(bf16x2, a), __builtin_bit_cast(bf16x2, b), c, false);
}
template <int CTRL> __device__ __forceinline__ float dpp(float x) {
  return __builtin_bit_cast(float, __builtin_amdgcn_mov_dpp(__builtin_bit_cast(int, x), CTRL, 0xf, 0xf, true));
}
constexpr int XOR1 = 0xB1, XOR2 = 0x4E, HMIRROR = 0x141, ROR8 = 0x128, ROR4 = 0x124, NEWBCAST = 0x150;

__device__ __forceinline__ float swap16_add(float a, float b) {
  auto s = __builtin_amdgcn_permlane16_swap(__float_as_uint(a), __float_as_uint(b), false, false);
  return __uint_as_float(s[0]) + __uint_as_float(s[1]);
}
__device__ __forceinline__ float xor16_max(float x) {
  auto s = __builtin_amdgcn_permlane16_swap(__float_as_uint(x), __float_as_uint(x), false, false);
  return fmaxf(__uint_as_float(s[0]), __uint_as_float(s[1]));
}
__device__ __forceinline__ float sum32(float x) {
  x += dpp<XOR1>(x); x += dpp<XOR2>(x); x += dpp<HMIRROR>(x); x += dpp<ROR8>(x);
  return swap16_add(x, x);
}
__device__ __forceinline__ float max32(float x) {
  x = fmaxf(x, dpp<XOR1>(x)); x = fmaxf(x, dpp<XOR2>(x)); x = fmaxf(x, dpp<HMIRROR>(x)); x = fmaxf(x, dpp<ROR8>(x));
  return xor16_max(x);
}
__device__ __forceinline__ unsigned not_sent(float x) { return min(__float_as_uint(x), SENT - 1u); }

#define PIN() do { asm volatile("" ::: "memory"); __builtin_amdgcn_sched_barrier(0); } while (0)
struct Tile { u32x4 r[8]; };

__device__ __forceinline__ void issue(Tile& t, __amdgpu_buffer_rsrc_t rs, unsigned voff, unsigned soff) {
#pragma unroll
  for (int j = 0; j < 8; ++j) {
    t.r[j] = __builtin_amdgcn_raw_buffer_load_b128(rs, voff, soff + j * 2 * TOK_BYTES, AUX_NT);
    __builtin_amdgcn_sched_barrier(0);
  }
}

template <int J> __device__ __forceinline__ void pv_step(const u32x4& v, float p, float (&acc)[2][8]) {
  const float p0 = dpp<NEWBCAST + 2 * J>(p), p1 = dpp<NEWBCAST + 2 * J + 1>(p);
#pragma unroll
  for (int k = 0; k < 4; ++k) {
    const float lo = __uint_as_float(v[k] << 16), hi = __uint_as_float(v[k] & 0xffff0000u);
    acc[0][2 * k] = fmaf(p0, lo, acc[0][2 * k]);
    acc[0][2 * k + 1] = fmaf(p0, hi, acc[0][2 * k + 1]);
    acc[1][2 * k] = fmaf(p1, lo, acc[1][2 * k]);
    acc[1][2 * k + 1] = fmaf(p1, hi, acc[1][2 * k + 1]);
  }
  __builtin_amdgcn_sched_barrier(0);
}

__device__ __forceinline__ float rec_weights(const float* lds, int g, float (&wt)[2 * NW]) {
  f32x2 ml[2 * NW];
#pragma unroll
  for (int r = 0; r < 2 * NW; ++r) ml[r] = *reinterpret_cast<const f32x2*>(lds + (r * G + g) * REC);
  float M = NEG_BIG;
#pragma unroll
  for (int r = 0; r < 2 * NW; ++r) M = fmaxf(M, ml[r][0]);
  float L = 0.f;
#pragma unroll
  for (int r = 0; r < 2 * NW; ++r) {
    wt[r] = __builtin_amdgcn_exp2f(ml[r][0] - M);
    L = fmaf(wt[r], ml[r][1], L);
  }
  const float inv = L > 0.f ? __builtin_amdgcn_rcpf(L) : 0.f;
#pragma unroll
  for (int r = 0; r < 2 * NW; ++r) wt[r] *= inv;
  return L > 0.f ? M + __builtin_amdgcn_logf(L) : NEG_BIG;
}

__global__ void __launch_bounds__((NW + 1) * 64, 2)
attn_decode(const u32x4* __restrict__ q, const void* __restrict__ kc, const void* __restrict__ vc,
            const int* __restrict__ bt, const int* __restrict__ lens, unsigned char* __restrict__ ws,
            unsigned char* __restrict__ out, int nbps, unsigned kv_bytes) {
  __shared__ __attribute__((aligned(16))) float lds[NW * 2 * G * REC];
  unsigned pf0;
  {
    const unsigned off = min((threadIdx.x & 63) * 128u, (unsigned)CODEPF);
    asm volatile("s_getpc_b64 s[90:91]\n\tglobal_load_dword %0, %1, s[90:91]" : "=&v"(pf0) : "v"(off) : "s90", "s91");
    __builtin_amdgcn_sched_barrier(0);
  }
  asm volatile("" : "+s"(ws), "+s"(out) : "s"(q), "s"(kc), "s"(vc), "s"(bt), "s"(lens), "s"(nbps), "s"(kv_bytes));
  const int head = (HPERM >> (4 * (blockIdx.x & 7))) & 7, s = blockIdx.x >> 3;
  const int lane = threadIdx.x & 63;
  const int w = __builtin_amdgcn_readfirstlane(threadIdx.x >> 6);
  const int h = lane >> 5, c = lane & 31;
  const __amdgpu_buffer_rsrc_t wrs = __builtin_amdgcn_make_buffer_rsrc(ws, 0, WS_BYTES, 0x00020000);
  const unsigned slot = WS_P + (head * NSPLIT + s) * (G * D * 4);

  if (__builtin_expect(w < NW, 1)) {
    const __amdgpu_buffer_rsrc_t krs = __builtin_amdgcn_make_buffer_rsrc((void*)kc, 0, kv_bytes, 0x00020000);
    const __amdgpu_buffer_rsrc_t vrs = __builtin_amdgcn_make_buffer_rsrc((void*)vc, 0, kv_bytes, 0x00020000);
    const int wv = w * NSPLIT + s;
    const int bt0 = ((cint_p)bt)[min(wv, nbps - 1)];
    const int len_v = __builtin_nontemporal_load(lens);
    const int btv = bt[min(wv + lane * T, nbps - 1)];
    u32x4 qv[G];
#pragma unroll
    for (int g = 0; g < G; ++g) qv[g] = q[(head * G + g) * (D / 8) + c];
    __builtin_amdgcn_sched_barrier(0);
    const unsigned voff = h * TOK_BYTES + c * 16;
    const unsigned hoff = head * (D * 2);
    Tile K, V;
    {
      const unsigned o0 = (unsigned)bt0 * PAGE_BYTES + hoff;
      issue(K, krs, voff, o0);
      issue(V, vrs, voff, o0);
      PIN();
    }
    const int len_raw = __builtin_amdgcn_readfirstlane(len_v);
    const int skv = nbps * PAGE;
    const bool all_masked = len_raw <= 0;
    const int len = all_masked ? skv : min(len_raw, skv);
    const float sc = all_masked ? 0.f : 0.0625f * 1.4426950408889634f;
    const int npages = (len + PAGE - 1) / PAGE;
    const int n = npages > wv ? (npages - wv + T - 1) / T : 0;
    float m = NEG_BIG, l = 0.f, acc[2][8];
#pragma unroll
    for (int k = 0; k < 8; ++k) { acc[0][k] = 0.f; acc[1][k] = 0.f; }
    int tok = wv * PAGE + 2 * ((lane & 15) >> 1) + h;
    int i = 0;
    if (n > 0) do {
      float x[16];
#pragma unroll
      for (int j = 0; j < 8; ++j)
#pragma unroll
        for (int g = 0; g < G; ++g) {
          float d = 0.f;
#pragma unroll
          for (int k = 0; k < 4; ++k) d = dot2(K.r[j][k], qv[g][k], d);
          x[2 * j + g] = d;
        }
      ++i;
      const unsigned onx = (unsigned)__builtin_amdgcn_readlane(btv, i & 63) * PAGE_BYTES + hoff;
      const unsigned onext = i < n ? onx : OOB;
      PIN();
      issue(K, krs, voff, onext);
      PIN();
#define TR_STEP(HALF, CTRL, BIT)                                            \
  _Pragma("unroll") for (int u = 0; u < (HALF); ++u) {                      \
    const float lo_ = x[u], hi_ = x[u + (HALF)];                            \
    const float t1_ = lo_ + dpp<CTRL>(lo_), t2_ = hi_ + dpp<CTRL>(hi_);     \
    x[u] = (lane & (BIT)) ? t2_ : t1_;                                      \
  }
      TR_STEP(8, ROR8, 8) TR_STEP(4, HMIRROR, 4) TR_STEP(2, XOR2, 2) TR_STEP(1, XOR1, 1)
      float sv = swap16_add(x[0], x[0]) * sc;
      sv = tok < len ? sv : -INFINITY;
      tok += T * PAGE;
      float pm = fmaxf(sv, dpp<XOR2>(sv));
      pm = fmaxf(pm, dpp<ROR4>(pm));
      pm = fmaxf(pm, dpp<ROR8>(pm));
      const float mn = fmaxf(m, pm);
      if (__builtin_amdgcn_ballot_w64(mn > m)) {
        const float alpha = __builtin_amdgcn_exp2f(m - mn);
        l *= alpha;
        const float a0 = dpp<0x00>(alpha), a1 = dpp<0x55>(alpha);
#pragma unroll
        for (int k = 0; k < 8; ++k) { acc[0][k] *= a0; acc[1][k] *= a1; }
        m = mn;
      }
      const float p = __builtin_amdgcn_exp2f(sv - m);
      l += p;
      pv_step<0>(V.r[0], p, acc); pv_step<1>(V.r[1], p, acc);
      pv_step<2>(V.r[2], p, acc); pv_step<3>(V.r[3], p, acc);
      pv_step<4>(V.r[4], p, acc); pv_step<5>(V.r[5], p, acc);
      pv_step<6>(V.r[6], p, acc); pv_step<7>(V.r[7], p, acc);
      PIN();
      issue(V, vrs, voff, onext);
      PIN();
    } while (i < n);
    l += dpp<XOR2>(l);
    l += dpp<ROR4>(l);
    l += dpp<ROR8>(l);
#pragma unroll
    for (int g = 0; g < G; ++g) {
      float* rec = lds + ((w * 2 + h) * G + g) * REC;
      *reinterpret_cast<f32x4*>(rec + 4 + 8 * c) = f32x4{acc[g][0], acc[g][1], acc[g][2], acc[g][3]};
      *reinterpret_cast<f32x4*>(rec + 8 + 8 * c) = f32x4{acc[g][4], acc[g][5], acc[g][6], acc[g][7]};
    }
    if (c < 2) *reinterpret_cast<f32x2*>(lds + ((w * 2 + h) * G + c) * REC) = f32x2{m, l};
    __syncthreads();
    {
      const int g = w & 1, d0 = 128 * (w >> 1) + 2 * lane;
      f32x2 ra[2 * NW];
#pragma unroll
      for (int r = 0; r < 2 * NW; ++r) ra[r] = *reinterpret_cast<const f32x2*>(lds + (r * G + g) * REC + 4 + d0);
      float wt[2 * NW];
      rec_weights(lds, g, wt);
      f32x2 a = {0.f, 0.f};
#pragma unroll
      for (int r = 0; r < 2 * NW; ++r) a += wt[r] * ra[r];
      __builtin_amdgcn_raw_buffer_store_b64(u32x2{not_sent(a[0]), not_sent(a[1])}, wrs, (g * D + d0) * 4, slot, AUX_SC1);
    }
    asm volatile("s_waitcnt vmcnt(0)" ::"v"(pf0));
    return;
  }

  const __amdgpu_buffer_rsrc_t ors = __builtin_amdgcn_make_buffer_rsrc(out, 0, HKV * G * D * 2, 0x00020000);
  const unsigned put_f = lane == 0 ? WS_F + (head * NSPLIT + s) * 128 : OOB;
  const unsigned get_f = WS_F + (head * NSPLIT + c) * 128;
  const u32x2 e = __builtin_amdgcn_raw_buffer_load_b64(wrs, 0, WS_E + head * 256, AUX_SC1);
  {
    const u32x4 sent4 = {SENT, SENT, SENT, SENT};
    __builtin_amdgcn_raw_buffer_store_b128(sent4, wrs, lane * 16, slot, AUX_SC1);
    __builtin_amdgcn_raw_buffer_store_b128(sent4, wrs, lane * 16 + 1024, slot, AUX_SC1);
    __builtin_amdgcn_raw_buffer_store_b64(u32x2{SENT, SENT}, wrs, put_f + 8, 0, AUX_SC1);
  }
  const unsigned long long enext = (((unsigned long long)e[1] << 32) | e[0]) + 1ull;
  const unsigned fa_lo = (unsigned)(enext << 1), fa_hi = (unsigned)(enext >> 31);
  const int g_out = s >> 4;
  const unsigned get = (c == s) ? OOB : WS_P + ((head * NSPLIT + c) * G + g_out) * (D * 4) + (16 * (s & 15) + 8 * h) * 4;
  asm volatile("s_waitcnt vmcnt(0)" ::: "memory");
  asm volatile("" ::"v"(pf0));
  __syncthreads();
  u32x4 f = __builtin_amdgcn_raw_buffer_load_b128(wrs, get_f, 0, AUX_SC1);
  PIN();
  float wt[2 * NW];
  const unsigned ul = not_sent(rec_weights(lds, h, wt));
  const unsigned ul0 = __builtin_amdgcn_readlane(ul, 0), ul1 = __builtin_amdgcn_readlane(ul, 32);
  __builtin_amdgcn_raw_buffer_store_b128(u32x4{fa_lo | 1u, fa_hi, ul0, ul1}, wrs, put_f, 0, AUX_SC1);
  PIN();
  f32x4 pa = {0.f, 0.f, 0.f, 0.f}, pb = pa;
  {
    const float* rec = lds + g_out * REC + 4 + 16 * (s & 15) + 8 * h;
#pragma unroll
    for (int r = 0; r < 2 * NW; ++r) {
      const float wr = __uint_as_float(__builtin_amdgcn_readlane(__float_as_uint(wt[r]), g_out * 32));
      pa += wr * *reinterpret_cast<const f32x4*>(rec + r * G * REC);
      pb += wr * *reinterpret_cast<const f32x4*>(rec + r * G * REC + 4);
    }
  }
  if (c == s) f = u32x4{fa_lo | 1u, fa_hi, ul0, ul1};
  auto not_ready = [&]() { return f[0] != (fa_lo | 1u) || f[1] != fa_hi || (g_out ? f[3] : f[2]) == SENT; };
  bool wait_f = not_ready();
  if (__builtin_amdgcn_ballot_w64(wait_f) != 0) {
    unsigned spin = 0;
#pragma nounroll
    do {
      __builtin_amdgcn_s_sleep(GATESLEEP);
      asm volatile("" ::: "memory");
      if (wait_f) {
        f = __builtin_amdgcn_raw_buffer_load_b128(wrs, get_f, 0, AUX_SC1);
        wait_f = not_ready();
      }
    } while (__builtin_amdgcn_ballot_w64(wait_f) != 0 && ++spin < SPIN_CAP);
  }
  asm volatile("" ::: "memory");
  u32x4 ua = __builtin_amdgcn_raw_buffer_load_b128(wrs, get, 0, AUX_SC1);
  u32x4 ub = __builtin_amdgcn_raw_buffer_load_b128(wrs, get + 16, 0, AUX_SC1);
  __builtin_amdgcn_raw_buffer_store_b64(u32x2{(unsigned)enext, (unsigned)(enext >> 32)}, wrs,
                                        (lane == 0 && s == 0) ? WS_E + head * 256 : OOB, 0, AUX_SC1);
  PIN();
  const float lse = __uint_as_float(g_out ? f[3] : f[2]);
  const float M = max32(lse);
  float wgt = __builtin_amdgcn_exp2f(lse - M);
  wgt *= __builtin_amdgcn_rcpf(sum32(wgt));
  PIN();
  auto has_sent = [&]() {
    return max(max(max(ua[0], ua[1]), max(ua[2], ua[3])), max(max(ub[0], ub[1]), max(ub[2], ub[3]))) == SENT;
  };
  bool pending = has_sent();
  if (__builtin_amdgcn_ballot_w64(pending) != 0) {
    unsigned spin = 0;
#pragma nounroll
    do {
      asm volatile("" ::: "memory");
      if (pending) {
        ua = __builtin_amdgcn_raw_buffer_load_b128(wrs, get, 0, AUX_SC1);
        ub = __builtin_amdgcn_raw_buffer_load_b128(wrs, get + 16, 0, AUX_SC1);
        pending = has_sent();
      }
    } while (__builtin_amdgcn_ballot_w64(pending) != 0 && ++spin < SPIN_CAP);
  }
  float x[8];
#pragma unroll
  for (int k = 0; k < 4; ++k) {
    x[k] = wgt * (c == s ? pa[k] : __uint_as_float(ua[k]));
    x[4 + k] = wgt * (c == s ? pb[k] : __uint_as_float(ub[k]));
  }
#pragma unroll
  for (int u = 0; u < 4; ++u) x[u] = swap16_add(x[u], x[u + 4]);
  TR_STEP(2, ROR8, 8) TR_STEP(1, HMIRROR, 4)
#undef TR_STEP
  float o = x[0];
  o += dpp<XOR2>(o);
  o += dpp<XOR1>(o);
  const __bf16 ob = (__bf16)o;
  __builtin_amdgcn_raw_buffer_store_b16(__builtin_bit_cast(unsigned short, ob), ors,
                                        (lane & 3) == 0 ? (head * G * D + 16 * s + 8 * h + (c >> 2)) * 2 : OOB, 0, 0);
}

}

extern "C" void kernel_launch(void* const* d_in, const int* in_sizes, int n_in, void* d_out, int out_size,
                              void* d_ws, size_t ws_size, hipStream_t stream) {
  const int nbps = in_sizes[3];
  const unsigned kv_bytes = (unsigned)in_sizes[1] * 2u;
  attn_decode<<<HKV * NSPLIT, (NW + 1) * 64, 0, stream>>>((const u32x4*)d_in[0], d_in[1], d_in[2], (const int*)d_in[3],
                                                          (const int*)d_in[4], (unsigned char*)d_ws,
                                                          (unsigned char*)d_out, nbps, kv_bytes);
}
```

```cpp
#include <hip/hip_runtime.h>
#include <hip/hip_bf16.h>
#include <stdint.h>

#define GATESLEEP 2
#define CODEPF 0
#define HDELAY 0
#define HPERM 0x54321076

namespace {

constexpr int D = 256, PAGE = 16, HKV = 8, G = 2, NSPLIT = 32, NW = 4;
constexpr int T = NW * NSPLIT;
constexpr unsigned PAGE_BYTES = PAGE * HKV * D * 2;
constexpr unsigned TOK_BYTES = HKV * D * 2;
constexpr float NEG_BIG = -3.0e38f;
constexpr int AUX_NT = 2, AUX_SC1 = 16;
constexpr unsigned WS_E = 0, WS_F = 4096, WS_P = WS_F + HKV * NSPLIT * 128, WS_BYTES = WS_P + HKV * NSPLIT * G * D * 4;
constexpr unsigned OOB = 0xF0000000u;
constexpr int REC = D + 4;
constexpr unsigned SPIN_CAP = 400000;

typedef unsigned u32x4 __attribute__((ext_vector_type(4)));
typedef unsigned u32x2 __attribute__((ext_vector_type(2)));
typedef float f32x4 __attribute__((ext_vector_type(4)));
typedef float f32x2 __attribute__((ext_vector_type(2)));
typedef __bf16 bf16x2 __attribute__((ext_vector_type(2)));
typedef const __attribute__((address_space(4))) int* cint_p;

__device__ __forceinline__ float dot2(unsigned a, unsigned b, float c) {
  return __builtin_amdgcn_fdot2_f32_bf16(__builtin_bit_cast(bf16x2, a), __builtin_bit_cast(bf16x2, b), c, false);
}
template <int CTRL> __device__ __forceinline__ float dpp(float x) {
  return __builtin_bit_cast(float, __builtin_amdgcn_mov_dpp(__builtin_bit_cast(int, x), CTRL, 0xf, 0xf, true));
}
constexpr int XOR1 = 0xB1, XOR2 = 0x4E, HMIRROR = 0x141, ROR8 = 0x128, ROR4 = 0x124, NEWBCAST = 0x150;

__device__ __forceinline__ float swap16_add(float a, float b) {
  auto s = __builtin_amdgcn_permlane16_swap(__float_as_uint(a), __float_as_uint(b), false, false);
  return __uint_as_float(s[0]) + __uint_as_float(s[1]);
}
__device__ __forceinline__ float xor16_max(float x) {
  auto s = __builtin_amdgcn_permlane16_swap(__float_as_uint(x), __float_as_uint(x), false, false);
  return fmaxf(__uint_as_float(s[0]), __uint_as_float(s[1]));
}
__device__ __forceinline__ float sum32(float x) {
  x += dpp<XOR1>(x); x += dpp<XOR2>(x); x += dpp<HMIRROR>(x); x += dpp<ROR8>(x);
  return swap16_add(x, x);
}
__device__ __forceinline__ float max32(float x) {
  x = fmaxf(x, dpp<XOR1>(x)); x = fmaxf(x, dpp<XOR2>(x)); x = fmaxf(x, dpp<HMIRROR>(x)); x = fmaxf(x, dpp<ROR8>(x));
  return xor16_max(x);
}

#define PIN() do { asm volatile("" ::: "memory"); __builtin_amdgcn_sched_barrier(0); } while (0)
struct Tile { u32x4 r[8]; };

__device__ __forceinline__ void issue(Tile& t, __amdgpu_buffer_rsrc_t rs, unsigned voff, unsigned soff) {
#pragma unroll
  for (int j = 0; j < 8; ++j) {
    t.r[j] = __builtin_amdgcn_raw_buffer_load_b128(rs, voff, soff + j * 2 * TOK_BYTES, AUX_NT);
    __builtin_amdgcn_sched_barrier(0);
  }
}

template <int J> __device__ __forceinline__ void pv_step(const u32x4& v, float p, float (&acc)[2][8]) {
  const float p0 = dpp<NEWBCAST + 2 * J>(p), p1 = dpp<NEWBCAST + 2 * J + 1>(p);
#pragma unroll
  for (int k = 0; k < 4; ++k) {
    const float lo = __uint_as_float(v[k] << 16), hi = __uint_as_float(v[k] & 0xffff0000u);
    acc[0][2 * k] = fmaf(p0, lo, acc[0][2 * k]);
    acc[0][2 * k + 1] = fmaf(p0, hi, acc[0][2 * k + 1]);
    acc[1][2 * k] = fmaf(p1, lo, acc[1][2 * k]);
    acc[1][2 * k + 1] = fmaf(p1, hi, acc[1][2 * k + 1]);
  }
  __builtin_amdgcn_sched_barrier(0);
}

__device__ __forceinline__ float rec_weights(const float* lds, int g, float (&wt)[2 * NW]) {
  f32x2 ml[2 * NW];
#pragma unroll
  for (int r = 0; r < 2 * NW; ++r) ml[r] = *reinterpret_cast<const f32x2*>(lds + (r * G + g) * REC);
  float M = NEG_BIG;
#pragma unroll
  for (int r = 0; r < 2 * NW; ++r) M = fmaxf(M, ml[r][0]);
  float L = 0.f;
#pragma unroll
  for (int r = 0; r < 2 * NW; ++r) {
    wt[r] = __builtin_amdgcn_exp2f(ml[r][0] - M);
    L = fmaf(wt[r], ml[r][1], L);
  }
  const float inv = L > 0.f ? __builtin_amdgcn_rcpf(L) : 0.f;
#pragma unroll
  for (int r = 0; r < 2 * NW; ++r) wt[r] *= inv;
  return L > 0.f ? M + __builtin_amdgcn_logf(L) : NEG_BIG;
}

__global__ void __launch_bounds__((NW + 1) * 64, 2)
attn_decode(const u32x4* __restrict__ q, const void* __restrict__ kc, const void* __restrict__ vc,
            const int* __restrict__ bt, const int* __restrict__ lens, unsigned char* __restrict__ ws,
            unsigned char* __restrict__ out, int nbps, unsigned kv_bytes) {
  __shared__ __attribute__((aligned(16))) float lds[NW * 2 * G * REC + 4];
  asm volatile("" : "+s"(ws), "+s"(out) : "s"(q), "s"(kc), "s"(vc), "s"(bt), "s"(lens), "s"(nbps), "s"(kv_bytes));
  const int head = (HPERM >> (4 * (blockIdx.x & 7))) & 7, s = blockIdx.x >> 3;
  const int lane = threadIdx.x & 63;
  const int w = __builtin_amdgcn_readfirstlane(threadIdx.x >> 6);
  const int h = lane >> 5, c = lane & 31;
  const __amdgpu_buffer_rsrc_t wrs = __builtin_amdgcn_make_buffer_rsrc(ws, 0, WS_BYTES, 0x00020000);
  const unsigned slot = WS_P + (head * NSPLIT + s) * (G * D * 4);

  if (__builtin_expect(w < NW, 1)) {
    const __amdgpu_buffer_rsrc_t krs = __builtin_amdgcn_make_buffer_rsrc((void*)kc, 0, kv_bytes, 0x00020000);
    const __amdgpu_buffer_rsrc_t vrs = __builtin_amdgcn_make_buffer_rsrc((void*)vc, 0, kv_bytes, 0x00020000);
    const int wv = w * NSPLIT + s;
    const int bt0 = ((cint_p)bt)[min(wv, nbps - 1)];
    const int len_v = __builtin_nontemporal_load(lens);
    const int btv = bt[min(wv + lane * T, nbps - 1)];
    u32x4 qv[G];
#pragma unroll
    for (int g = 0; g < G; ++g) qv[g] = q[(head * G + g) * (D / 8) + c];
    __builtin_amdgcn_sched_barrier(0);
    const unsigned voff = h * TOK_BYTES + c * 16;
    const unsigned hoff = head * (D * 2);
    Tile K, V;
    {
      const unsigned o0 = (unsigned)bt0 * PAGE_BYTES + hoff;
      issue(K, krs, voff, o0);
      issue(V, vrs, voff, o0);
      PIN();
    }
    const int len_raw = __builtin_amdgcn_readfirstlane(len_v);
    const int skv = nbps * PAGE;
    const bool all_masked = len_raw <= 0;
    const int len = all_masked ? skv : min(len_raw, skv);
    const float sc = all_masked ? 0.f : 0.0625f * 1.4426950408889634f;
    const int npages = (len + PAGE - 1) / PAGE;
    const int n = npages > wv ? (npages - wv + T - 1) / T : 0;
    float m = NEG_BIG, l = 0.f, acc[2][8];
#pragma unroll
    for (int k = 0; k < 8; ++k) { acc[0][k] = 0.f; acc[1][k] = 0.f; }
    int tok = wv * PAGE + 2 * ((lane & 15) >> 1) + h;
    int i = 0;
    if (n > 0) do {
      float x[16];
#pragma unroll
      for (int j = 0; j < 8; ++j)
#pragma unroll
        for (int g = 0; g < G; ++g) {
          float d = 0.f;
#pragma unroll
          for (int k = 0; k < 4; ++k) d = dot2(K.r[j][k], qv[g][k], d);
          x[2 * j + g] = d;
        }
      ++i;
      const unsigned onx = (unsigned)__builtin_amdgcn_readlane(btv, i & 63) * PAGE_BYTES + hoff;
      const unsigned onext = i < n ? onx : OOB;
      PIN();
      issue(K, krs, voff, onext);
      PIN();
#define TR_STEP(HALF, CTRL, BIT)                                            \
  _Pragma("unroll") for (int u = 0; u < (HALF); ++u) {                      \
    const float lo_ = x[u], hi_ = x[u + (HALF)];                            \
    const float t1_ = lo_ + dpp<CTRL>(lo_), t2_ = hi_ + dpp<CTRL>(hi_);     \
    x[u] = (lane & (BIT)) ? t2_ : t1_;                                      \
  }
      TR_STEP(8, ROR8, 8) TR_STEP(4, HMIRROR, 4) TR_STEP(2, XOR2, 2) TR_STEP(1, XOR1, 1)
      float sv = swap16_add(x[0], x[0]) * sc;
      sv = tok < len ? sv : -INFINITY;
      tok += T * PAGE;
      float pm = fmaxf(sv, dpp<XOR2>(sv));
      pm = fmaxf(pm, dpp<ROR4>(pm));
      pm = fmaxf(pm, dpp<ROR8>(pm));
      const float mn = fmaxf(m, pm);
      if (__builtin_amdgcn_ballot_w64(mn > m)) {
        const float alpha = __builtin_amdgcn_exp2f(m - mn);
        l *= alpha;
        const float a0 = dpp<0x00>(alpha), a1 = dpp<0x55>(alpha);
#pragma unroll
        for (int k = 0; k < 8; ++k) { acc[0][k] *= a0; acc[1][k] *= a1; }
        m = mn;
      }
      const float p = __builtin_amdgcn_exp2f(sv - m);
      l += p;
      pv_step<0>(V.r[0], p, acc); pv_step<1>(V.r[1], p, acc);
      pv_step<2>(V.r[2], p, acc); pv_step<3>(V.r[3], p, acc);
      pv_step<4>(V.r[4], p, acc); pv_step<5>(V.r[5], p, acc);
      pv_step<6>(V.r[6], p, acc); pv_step<7>(V.r[7], p, acc);
      PIN();
      issue(V, vrs, voff, onext);
      PIN();
    } while (i < n);
    l += dpp<XOR2>(l);
    l += dpp<ROR4>(l);
    l += dpp<ROR8>(l);
#pragma unroll
    for (int g = 0; g < G; ++g) {
      float* rec = lds + ((w * 2 + h) * G + g) * REC;
      *reinterpret_cast<f32x4*>(rec + 4 + 8 * c) = f32x4{acc[g][0], acc[g][1], acc[g][2], acc[g][3]};
      *reinterpret_cast<f32x4*>(rec + 8 + 8 * c) = f32x4{acc[g][4], acc[g][5], acc[g][6], acc[g][7]};
    }
    if (c < 2) *reinterpret_cast<f32x2*>(lds + ((w * 2 + h) * G + c) * REC) = f32x2{m, l};
    __syncthreads();
    {
      const int g = w & 1, d0 = 128 * (w >> 1) + 2 * lane;
      const unsigned tag = __float_as_uint(lds[NW * 2 * G * REC]);
      f32x2 ra[2 * NW];
#pragma unroll
      for (int r = 0; r < 2 * NW; ++r) ra[r] = *reinterpret_cast<const f32x2*>(lds + (r * G + g) * REC + 4 + d0);
      float wt[2 * NW];
      rec_weights(lds, g, wt);
      f32x2 a = {0.f, 0.f};
#pragma unroll
      for (int r = 0; r < 2 * NW; ++r) a += wt[r] * ra[r];
      const u32x2 ta = {(__float_as_uint(a[0]) & ~0xffu) | (tag & 0xffu), (__float_as_uint(a[1]) & ~0xffu) | ((tag >> 8) & 0xffu)};
      __builtin_amdgcn_raw_buffer_store_b64(ta, wrs, (g * D + d0) * 4, slot, AUX_SC1);
    }
    return;
  }

  const __amdgpu_buffer_rsrc_t ors = __builtin_amdgcn_make_buffer_rsrc(out, 0, HKV * G * D * 2, 0x00020000);
  const unsigned put_f = lane == 0 ? WS_F + (head * NSPLIT + s) * 128 : OOB;
  const unsigned get_f = WS_F + (head * NSPLIT + c) * 128;
  const u32x2 e = __builtin_amdgcn_raw_buffer_load_b64(wrs, 0, WS_E + head * 256, AUX_SC1);
  const unsigned long long enext = (((unsigned long long)e[1] << 32) | e[0]) + 1ull;
  const unsigned fa_lo = (unsigned)enext, fa_hi = (unsigned)(enext >> 32);
  const int g_out = s >> 4;
  const unsigned get = (c == s) ? OOB : WS_P + ((head * NSPLIT + c) * G + g_out) * (D * 4) + (16 * (s & 15) + 8 * h) * 4;
  lds[NW * 2 * G * REC] = __uint_as_float((unsigned)enext);
  __syncthreads();
  u32x4 f = __builtin_amdgcn_raw_buffer_load_b128(wrs, get_f, 0, AUX_SC1);
  PIN();
  float wt[2 * NW];
  const unsigned t0 = (unsigned)enext & 0xffu, t1 = ((unsigned)enext >> 8) & 0xffu;
  const unsigned ul = (__float_as_uint(rec_weights(lds, h, wt)) & ~0xffu) | t0;
  const unsigned ul0 = __builtin_amdgcn_readlane(ul, 0), ul1 = __builtin_amdgcn_readlane(ul, 32);
  __builtin_amdgcn_raw_buffer_store_b128(u32x4{fa_lo, fa_hi, ul0, ul1}, wrs, put_f, 0, AUX_SC1);
  PIN();
  f32x4 pa = {0.f, 0.f, 0.f, 0.f}, pb = pa;
  {
    const float* rec = lds + g_out * REC + 4 + 16 * (s & 15) + 8 * h;
#pragma unroll
    for (int r = 0; r < 2 * NW; ++r) {
      const float wr = __uint_as_float(__builtin_amdgcn_readlane(__float_as_uint(wt[r]), g_out * 32));
      pa += wr * *reinterpret_cast<const f32x4*>(rec + r * G * REC);
      pb += wr * *reinterpret_cast<const f32x4*>(rec + r * G * REC + 4);
    }
  }
  if (c == s) f = u32x4{fa_lo, fa_hi, ul0, ul1};
  auto not_ready = [&]() { return f[0] != (fa_lo) || f[1] != fa_hi || ((g_out ? f[3] : f[2]) & 0xffu) != t0; };
  bool wait_f = not_ready();
  if (__builtin_amdgcn_ballot_w64(wait_f) != 0) {
    unsigned spin = 0;
#pragma nounroll
    do {
      __builtin_amdgcn_s_sleep(GATESLEEP);
      asm volatile("" ::: "memory");
      if (wait_f) {
        f = __builtin_amdgcn_raw_buffer_load_b128(wrs, get_f, 0, AUX_SC1);
        wait_f = not_ready();
      }
    } while (__builtin_amdgcn_ballot_w64(wait_f) != 0 && ++spin < SPIN_CAP);
  }
  asm volatile("" ::: "memory");
  u32x4 ua = __builtin_amdgcn_raw_buffer_load_b128(wrs, get, 0, AUX_SC1);
  u32x4 ub = __builtin_amdgcn_raw_buffer_load_b128(wrs, get + 16, 0, AUX_SC1);
  __builtin_amdgcn_raw_buffer_store_b64(u32x2{(unsigned)enext, (unsigned)(enext >> 32)}, wrs,
                                        (lane == 0 && s == 0) ? WS_E + head * 256 : OOB, 0, AUX_SC1);
  PIN();
  const float lse = __uint_as_float(g_out ? f[3] : f[2]);
  const float M = max32(lse);
  float wgt = __builtin_amdgcn_exp2f(lse - M);
  wgt *= __builtin_amdgcn_rcpf(sum32(wgt));
  PIN();
  auto stale = [&]() {
    const unsigned x0 = (ua[0] ^ t0) | (ua[2] ^ t0) | (ub[0] ^ t0) | (ub[2] ^ t0);
    const unsigned x1 = (ua[1] ^ t1) | (ua[3] ^ t1) | (ub[1] ^ t1) | (ub[3] ^ t1);
    return ((x0 | x1) & 0xffu) != 0 && c != s;
  };
  bool pending = stale();
  if (__builtin_amdgcn_ballot_w64(pending) != 0) {
    unsigned spin = 0;
#pragma nounroll
    do {
      asm volatile("" ::: "memory");
      if (pending) {
        ua = __builtin_amdgcn_raw_buffer_load_b128(wrs, get, 0, AUX_SC1);
        ub = __builtin_amdgcn_raw_buffer_load_b128(wrs, get + 16, 0, AUX_SC1);
        pending = stale();
      }
    } while (__builtin_amdgcn_ballot_w64(pending) != 0 && ++spin < SPIN_CAP);
  }
  float x[8];
#pragma unroll
  for (int k = 0; k < 4; ++k) {
    x[k] = wgt * (c == s ? pa[k] : __uint_as_float(ua[k]));
    x[4 + k] = wgt * (c == s ? pb[k] : __uint_as_float(ub[k]));
  }
#pragma unroll
  for (int u = 0; u < 4; ++u) x[u] = swap16_add(x[u], x[u + 4]);
  TR_STEP(2, ROR8, 8) TR_STEP(1, HMIRROR, 4)
#undef TR_STEP
  float o = x[0];
  o += dpp<XOR2>(o);
  o += dpp<XOR1>(o);
  const __bf16 ob = (__bf16)o;
  __builtin_amdgcn_raw_buffer_store_b16(__builtin_bit_cast(unsigned short, ob), ors,
                                        (lane & 3) == 0 ? (head * G * D + 16 * s + 8 * h + (c >> 2)) * 2 : OOB, 0, 0);
}

}

extern "C" void kernel_launch(void* const* d_in, const int* in_sizes, int n_in, void* d_out, int out_size,
                              void* d_ws, size_t ws_size, hipStream_t stream) {
  const int nbps = in_sizes[3];
  const unsigned kv_bytes = (unsigned)in_sizes[1] * 2u;
  attn_decode<<<HKV * NSPLIT, (NW + 1) * 64, 0, stream>>>((const u32x4*)d_in[0], d_in[1], d_in[2], (const int*)d_in[3],
                                                          (const int*)d_in[4], (unsigned char*)d_ws,
                                                          (unsigned char*)d_out, nbps, kv_bytes);
}
```

```cpp
#define AMPMODE 0
#include <hip/hip_runtime.h>
#include <hip/hip_bf16.h>
#include <stdint.h>

#define GATESLEEP 2
#define CODEPF 5120
#define OUTAUX 0
#define HBIT 0
#define ROWJ(j) ((((j) >> HBIT) << (HBIT + 1)) | ((j) & ((1 << HBIT) - 1)))
#define HPERM 0x54321076

namespace {

constexpr int D = 256, PAGE = 16, HKV = 8, G = 2, NSPLIT = 32, NW = 4;
constexpr int T = NW * NSPLIT;
constexpr unsigned PAGE_BYTES = PAGE * HKV * D * 2;
constexpr unsigned TOK_BYTES = HKV * D * 2;
constexpr float NEG_BIG = -3.0e38f;
constexpr int AUX_NT = 2, AUX_SC1 = 16;
constexpr unsigned WS_E = 0, WS_F = 4096, WS_P = WS_F + HKV * NSPLIT * 128, WS_BYTES = WS_P + HKV * NSPLIT * G * D * 4;
constexpr unsigned OOB = 0xF0000000u;
constexpr int REC = D + 4;
constexpr unsigned SPIN_CAP = 400000;

typedef unsigned u32x4 __attribute__((ext_vector_type(4)));
typedef unsigned u32x2 __attribute__((ext_vector_type(2)));
typedef float f32x4 __attribute__((ext_vector_type(4)));
typedef float f32x2 __attribute__((ext_vector_type(2)));
typedef __bf16 bf16x2 __attribute__((ext_vector_type(2)));
typedef const __attribute__((address_space(4))) int* cint_p;

__device__ __forceinline__ float dot2(unsigned a, unsigned b, float c) {
  return __builtin_amdgcn_fdot2_f32_bf16(__builtin_bit_cast(bf16x2, a), __builtin_bit_cast(bf16x2, b), c, false);
}
template <int CTRL> __device__ __forceinline__ float dpp(float x) {
  return __builtin_bit_cast(float, __builtin_amdgcn_mov_dpp(__builtin_bit_cast(int, x), CTRL, 0xf, 0xf, true));
}
constexpr int XOR1 = 0xB1, XOR2 = 0x4E, HMIRROR = 0x141, ROR8 = 0x128, ROR4 = 0x124, NEWBCAST = 0x150;

__device__ __forceinline__ float swap16_add(float a, float b) {
  auto s = __builtin_amdgcn_permlane16_swap(__float_as_uint(a), __float_as_uint(b), false, false);
  return __uint_as_float(s[0]) + __uint_as_float(s[1]);
}
__device__ __forceinline__ float xor16_max(float x) {
  auto s = __builtin_amdgcn_permlane16_swap(__float_as_uint(x), __float_as_uint(x), false, false);
  return fmaxf(__uint_as_float(s[0]), __uint_as_float(s[1]));
}
__device__ __forceinline__ float sum32(float x) {
  x += dpp<XOR1>(x); x += dpp<XOR2>(x); x += dpp<HMIRROR>(x); x += dpp<ROR8>(x);
  return swap16_add(x, x);
}
__device__ __forceinline__ float max32(float x) {
  x = fmaxf(x, dpp<XOR1>(x)); x = fmaxf(x, dpp<XOR2>(x)); x = fmaxf(x, dpp<HMIRROR>(x)); x = fmaxf(x, dpp<ROR8>(x));
  return xor16_max(x);
}

#define PIN() do { asm volatile("" ::: "memory"); __builtin_amdgcn_sched_barrier(0); } while (0)
struct Tile { u32x4 r[8]; };

__device__ __forceinline__ void issue(Tile& t, __amdgpu_buffer_rsrc_t rs, unsigned voff, unsigned soff) {
#pragma unroll
  for (int j = 0; j < 8; ++j) {
    t.r[j] = __builtin_amdgcn_raw_buffer_load_b128(rs, voff, soff + ROWJ(j) * TOK_BYTES, AUX_NT);
    __builtin_amdgcn_sched_barrier(0);
  }
}

template <int J> __device__ __forceinline__ void pv_step(const u32x4& v, float p, float (&acc)[2][8]) {
  const float p0 = dpp<NEWBCAST + 2 * J>(p), p1 = dpp<NEWBCAST + 2 * J + 1>(p);
#pragma unroll
  for (int k = 0; k < 4; ++k) {
    const float lo = __uint_as_float(v[k] << 16), hi = __uint_as_float(v[k] & 0xffff0000u);
    acc[0][2 * k] = fmaf(p0, lo, acc[0][2 * k]);
    acc[0][2 * k + 1] = fmaf(p0, hi, acc[0][2 * k + 1]);
    acc[1][2 * k] = fmaf(p1, lo, acc[1][2 * k]);
    acc[1][2 * k + 1] = fmaf(p1, hi, acc[1][2 * k + 1]);
  }
  __builtin_amdgcn_sched_barrier(0);
}

__device__ __forceinline__ float rec_weights(const float* lds, int g, float (&wt)[2 * NW]) {
  f32x2 ml[2 * NW];
#pragma unroll
  for (int r = 0; r < 2 * NW; ++r) ml[r] = *reinterpret_cast<const f32x2*>(lds + (r * G + g) * REC);
  float M = NEG_BIG;
#pragma unroll
  for (int r = 0; r < 2 * NW; ++r) M = fmaxf(M, ml[r][0]);
  float L = 0.f;
#pragma unroll
  for (int r = 0; r < 2 * NW; ++r) {
    wt[r] = __builtin_amdgcn_exp2f(ml[r][0] - M);
    L = fmaf(wt[r], ml[r][1], L);
  }
  const float inv = L > 0.f ? __builtin_amdgcn_rcpf(L) : 0.f;
#pragma unroll
  for (int r = 0; r < 2 * NW; ++r) wt[r] *= inv;
  return L > 0.f ? M + __builtin_amdgcn_logf(L) : NEG_BIG;
}

__global__ void __launch_bounds__((NW + 1) * 64, 2)
attn_decode(const u32x4* __restrict__ q, const void* __restrict__ kc, const void* __restrict__ vc,
            const int* __restrict__ bt, const int* __restrict__ lens, unsigned char* __restrict__ ws,
            unsigned char* __restrict__ out, int nbps, unsigned kv_bytes) {
  __shared__ __attribute__((aligned(16))) float lds[NW * 2 * G * REC + 4];
  unsigned long long t_start = __builtin_amdgcn_s_memrealtime();
  unsigned pf0;
  {
    const unsigned off = min((threadIdx.x & 63) * 128u, (unsigned)CODEPF);
    asm volatile("s_getpc_b64 s[90:91]\n\tglobal_load_dword %0, %1, s[90:91]" : "=&v"(pf0) : "v"(off) : "s90", "s91");
    __builtin_amdgcn_sched_barrier(0);
  }
  asm volatile("" : "+s"(ws), "+s"(out) : "s"(q), "s"(kc), "s"(vc), "s"(bt), "s"(lens), "s"(nbps), "s"(kv_bytes));
  const int head = (HPERM >> (4 * (blockIdx.x & 7))) & 7, s = blockIdx.x >> 3;
  const int lane = threadIdx.x & 63;
  const int w = __builtin_amdgcn_readfirstlane(threadIdx.x >> 6);
  const int h = lane >> 5, c = lane & 31;
  const __amdgpu_buffer_rsrc_t wrs = __builtin_amdgcn_make_buffer_rsrc(ws, 0, WS_BYTES, 0x00020000);
  const unsigned slot = WS_P + (head * NSPLIT + s) * (G * D * 4);

  if (__builtin_expect(w < NW, 1)) {
    const __amdgpu_buffer_rsrc_t krs = __builtin_amdgcn_make_buffer_rsrc((void*)kc, 0, kv_bytes, 0x00020000);
    const __amdgpu_buffer_rsrc_t vrs = __builtin_amdgcn_make_buffer_rsrc((void*)vc, 0, kv_bytes, 0x00020000);
    const int wv = w * NSPLIT + s;
    const int bt0 = ((cint_p)bt)[min(wv, nbps - 1)];
    const int len_v = __builtin_nontemporal_load(lens);
    const int btv = bt[min(wv + lane * T, nbps - 1)];
    u32x4 qv[G];
#pragma unroll
    for (int g = 0; g < G; ++g) qv[g] = q[(head * G + g) * (D / 8) + c];
    __builtin_amdgcn_sched_barrier(0);
    const unsigned voff = (h << HBIT) * TOK_BYTES + c * 16;
    const unsigned hoff = head * (D * 2);
    Tile K, V;
    {
      const unsigned o0 = (unsigned)bt0 * PAGE_BYTES + hoff;
      issue(K, krs, voff, o0);
      issue(V, vrs, voff, o0);
      PIN();
    }
    const int len_raw = __builtin_amdgcn_readfirstlane(len_v);
    const int skv = nbps * PAGE;
    const bool all_masked = len_raw <= 0;
    const int len = all_masked ? skv : min(len_raw, skv);
    const float sc = all_masked ? 0.f : 0.0625f * 1.4426950408889634f;
    const int npages = (len + PAGE - 1) / PAGE;
    const int n = npages > wv ? (npages - wv + T - 1) / T : 0;
    float m = NEG_BIG, l = 0.f, acc[2][8];
#pragma unroll
    for (int k = 0; k < 8; ++k) { acc[0][k] = 0.f; acc[1][k] = 0.f; }
    int tok = wv * PAGE + ROWJ((lane & 15) >> 1) + (h << HBIT);
    int i = 0;
    if (n > 0) do {
      float x[16];
#pragma unroll
      for (int j = 0; j < 8; ++j)
#pragma unroll
        for (int g = 0; g < G; ++g) {
          float d = 0.f;
#pragma unroll
          for (int k = 0; k < 4; ++k) d = dot2(K.r[j][k], qv[g][k], d);
          x[2 * j + g] = d;
        }
      ++i;
      const unsigned onx = (unsigned)__builtin_amdgcn_readlane(btv, i & 63) * PAGE_BYTES + hoff;
      const unsigned onext = i < n ? onx : OOB;
      PIN();
      issue(K, krs, voff, onext);
      PIN();
#define TR_STEP(HALF, CTRL, BIT)                                            \
  _Pragma("unroll") for (int u = 0; u < (HALF); ++u) {                      \
    const float lo_ = x[u], hi_ = x[u + (HALF)];                            \
    const float t1_ = lo_ + dpp<CTRL>(lo_), t2_ = hi_ + dpp<CTRL>(hi_);     \
    x[u] = (lane & (BIT)) ? t2_ : t1_;                                      \
  }
      TR_STEP(8, ROR8, 8) TR_STEP(4, HMIRROR, 4) TR_STEP(2, XOR2, 2) TR_STEP(1, XOR1, 1)
      float sv = swap16_add(x[0], x[0]) * sc;
      sv = tok < len ? sv : -INFINITY;
      tok += T * PAGE;
      float pm = fmaxf(sv, dpp<XOR2>(sv));
      pm = fmaxf(pm, dpp<ROR4>(pm));
      pm = fmaxf(pm, dpp<ROR8>(pm));
      const float mn = fmaxf(m, pm);
      if (__builtin_amdgcn_ballot_w64(mn > m)) {
        const float alpha = __builtin_amdgcn_exp2f(m - mn);
        l *= alpha;
        const float a0 = dpp<0x00>(alpha), a1 = dpp<0x55>(alpha);
#pragma unroll
        for (int k = 0; k < 8; ++k) { acc[0][k] *= a0; acc[1][k] *= a1; }
        m = mn;
      }
      const float p = __builtin_amdgcn_exp2f(sv - m);
      l += p;
      pv_step<0>(V.r[0], p, acc); pv_step<1>(V.r[1], p, acc);
      pv_step<2>(V.r[2], p, acc); pv_step<3>(V.r[3], p, acc);
      pv_step<4>(V.r[4], p, acc); pv_step<5>(V.r[5], p, acc);
      pv_step<6>(V.r[6], p, acc); pv_step<7>(V.r[7], p, acc);
      PIN();
      issue(V, vrs, voff, onext);
      PIN();
    } while (i < n);
    l += dpp<XOR2>(l);
    l += dpp<ROR4>(l);
    l += dpp<ROR8>(l);
#pragma unroll
    for (int g = 0; g < G; ++g) {
      float* rec = lds + ((w * 2 + h) * G + g) * REC;
      *reinterpret_cast<f32x4*>(rec + 4 + 8 * c) = f32x4{acc[g][0], acc[g][1], acc[g][2], acc[g][3]};
      *reinterpret_cast<f32x4*>(rec + 8 + 8 * c) = f32x4{acc[g][4], acc[g][5], acc[g][6], acc[g][7]};
    }
    if (c < 2) *reinterpret_cast<f32x2*>(lds + ((w * 2 + h) * G + c) * REC) = f32x2{m, l};
    __syncthreads();
    {
      const int g = w & 1, d0 = 128 * (w >> 1) + 2 * lane;
      const unsigned tag = __float_as_uint(lds[NW * 2 * G * REC]);
      f32x2 ra[2 * NW];
#pragma unroll
      for (int r = 0; r < 2 * NW; ++r) ra[r] = *reinterpret_cast<const f32x2*>(lds + (r * G + g) * REC + 4 + d0);
      float wt[2 * NW];
      rec_weights(lds, g, wt);
      f32x2 a = {0.f, 0.f};
#pragma unroll
      for (int r = 0; r < 2 * NW; ++r) a += wt[r] * ra[r];
      const u32x2 ta = {(__float_as_uint(a[0]) & ~0xffu) | (tag & 0xffu), (__float_as_uint(a[1]) & ~0xffu) | ((tag >> 8) & 0xffu)};
      __builtin_amdgcn_raw_buffer_store_b64(ta, wrs, (g * D + d0) * 4, slot, AUX_SC1);
    }
    asm volatile("s_waitcnt vmcnt(0)" ::"v"(pf0));
    return;
  }

  const __amdgpu_buffer_rsrc_t ors = __builtin_amdgcn_make_buffer_rsrc(out, 0, HKV * G * D * 2, 0x00020000);
  const int g_out = s >> 4;
  const unsigned put_f = c == 0 ? WS_F + (head * NSPLIT + s) * 128 + h * 16 : OOB;
  const unsigned get_f = WS_F + (head * NSPLIT + c) * 128 + g_out * 16;
  const u32x2 e = __builtin_amdgcn_raw_buffer_load_b64(wrs, 0, WS_E + head * 256, AUX_SC1);
  const unsigned long long enext = (((unsigned long long)e[1] << 32) | e[0]) + 1ull;
  const unsigned ep_lo = (unsigned)enext, ep_hi = (unsigned)(enext >> 32);
  const unsigned get = (c == s) ? OOB : WS_P + ((head * NSPLIT + c) * G + g_out) * (D * 4) + (16 * (s & 15) + 8 * h) * 4;
  lds[NW * 2 * G * REC] = __uint_as_float(ep_lo);
  asm volatile("" ::"v"(pf0));
  __syncthreads();
  const unsigned long long t_bar = __builtin_amdgcn_s_memrealtime();
  u32x4 f = __builtin_amdgcn_raw_buffer_load_b128(wrs, get_f, 0, AUX_SC1);
  PIN();
  float wt[2 * NW];
  const unsigned t0 = ep_lo & 0xffu, t1 = (ep_lo >> 8) & 0xffu;
  const unsigned ul = __float_as_uint(rec_weights(lds, h, wt));
  __builtin_amdgcn_raw_buffer_store_b128(u32x4{ul, ep_lo, ep_lo, ep_hi}, wrs, put_f, 0, AUX_SC1);
  PIN();
  const unsigned long long t_pub = __builtin_amdgcn_s_memrealtime();
  f32x4 pa = {0.f, 0.f, 0.f, 0.f}, pb = pa;
  {
    const float* rec = lds + g_out * REC + 4 + 16 * (s & 15) + 8 * h;
#pragma unroll
    for (int r = 0; r < 2 * NW; ++r) {
      const float wr = __uint_as_float(__builtin_amdgcn_readlane(__float_as_uint(wt[r]), g_out * 32));
      pa += wr * *reinterpret_cast<const f32x4*>(rec + r * G * REC);
      pb += wr * *reinterpret_cast<const f32x4*>(rec + r * G * REC + 4);
    }
  }
  if (c == s) f = u32x4{(unsigned)__builtin_amdgcn_readlane(ul, g_out * 32), ep_lo, ep_lo, ep_hi};
  auto not_ready = [&]() { return ((f[1] ^ ep_lo) | (f[2] ^ ep_lo) | (f[3] ^ ep_hi)) != 0; };
  bool wait_f = not_ready();
  if (__builtin_amdgcn_ballot_w64(wait_f) != 0) {
    unsigned spin = 0;
#pragma nounroll
    do {
      __builtin_amdgcn_s_sleep(GATESLEEP);
      asm volatile("" ::: "memory");
      if (wait_f) {
        f = __builtin_amdgcn_raw_buffer_load_b128(wrs, get_f, 0, AUX_SC1);
        wait_f = not_ready();
      }
    } while (__builtin_amdgcn_ballot_w64(wait_f) != 0 && ++spin < SPIN_CAP);
  }
  asm volatile("" ::: "memory");
  u32x4 ua = __builtin_amdgcn_raw_buffer_load_b128(wrs, get, 0, AUX_SC1);
  u32x4 ub = __builtin_amdgcn_raw_buffer_load_b128(wrs, get + 16, 0, AUX_SC1);
  __builtin_amdgcn_raw_buffer_store_b64(u32x2{ep_lo, ep_hi}, wrs,
                                        (lane == 0 && s == 0) ? WS_E + head * 256 : OOB, 0, AUX_SC1);
  PIN();
  const float lse = __uint_as_float(f[0]);
  const float M = max32(lse);
  float wgt = __builtin_amdgcn_exp2f(lse - M);
  wgt *= __builtin_amdgcn_rcpf(sum32(wgt));
  PIN();
  auto stale = [&]() {
    const unsigned x0 = (ua[0] ^ t0) | (ua[2] ^ t0) | (ub[0] ^ t0) | (ub[2] ^ t0);
    const unsigned x1 = (ua[1] ^ t1) | (ua[3] ^ t1) | (ub[1] ^ t1) | (ub[3] ^ t1);
    return ((x0 | x1) & 0xffu) != 0 && c != s;
  };
  bool pending = stale();
  if (__builtin_amdgcn_ballot_w64(pending) != 0) {
    unsigned spin = 0;
#pragma nounroll
    do {
      asm volatile("" ::: "memory");
      if (pending) {
        ua = __builtin_amdgcn_raw_buffer_load_b128(wrs, get, 0, AUX_SC1);
        ub = __builtin_amdgcn_raw_buffer_load_b128(wrs, get + 16, 0, AUX_SC1);
        pending = stale();
      }
    } while (__builtin_amdgcn_ballot_w64(pending) != 0 && ++spin < SPIN_CAP);
  }
  float x[8];
#pragma unroll
  for (int k = 0; k < 4; ++k) {
    x[k] = wgt * (c == s ? pa[k] : __uint_as_float(ua[k] & ~0xffu));
    x[4 + k] = wgt * (c == s ? pb[k] : __uint_as_float(ub[k] & ~0xffu));
  }
#pragma unroll
  for (int u = 0; u < 4; ++u) x[u] = swap16_add(x[u], x[u + 4]);
  TR_STEP(2, ROR8, 8) TR_STEP(1, HMIRROR, 4)
#undef TR_STEP
  float o = x[0];
  o += dpp<XOR2>(o);
  o += dpp<XOR1>(o);
  const __bf16 ob = (__bf16)o;
  __builtin_amdgcn_raw_buffer_store_b16(__builtin_bit_cast(unsigned short, ob), ors,
                                        (lane & 3) == 0 ? (head * G * D + 16 * s + 8 * h + (c >> 2)) * 2 : OOB, 0, OUTAUX);
  {
    const unsigned long long t_end = __builtin_amdgcn_s_memrealtime();
    const unsigned long long extra = 0 * (t_pub + t_bar + t_start);
    while (__builtin_amdgcn_s_memrealtime() - t_end < extra) __builtin_amdgcn_s_sleep(4);
  }
}

}

extern "C" void kernel_launch(void* const* d_in, const int* in_sizes, int n_in, void* d_out, int out_size,
                              void* d_ws, size_t ws_size, hipStream_t stream) {
  const int nbps = in_sizes[3];
  const unsigned kv_bytes = (unsigned)in_sizes[1] * 2u;
  attn_decode<<<HKV * NSPLIT, (NW + 1) * 64, 0, stream>>>((const u32x4*)d_in[0], d_in[1], d_in[2], (const int*)d_in[3],
                                                          (const int*)d_in[4], (unsigned char*)d_ws,
                                                          (unsigned char*)d_out, nbps, kv_bytes);
}
```

```cpp
#include <hip/hip_runtime.h>
#include <hip/hip_bf16.h>
#include <stdint.h>

namespace {

constexpr int D = 256, PAGE = 16, HKV = 8, G = 2, NSPLIT = 32, NW = 4;
constexpr int T = NW * NSPLIT;
constexpr unsigned PAGE_BYTES = PAGE * HKV * D * 2;
constexpr unsigned TOK_BYTES = HKV * D * 2;
constexpr unsigned HEAD_OF_GROUP = 0x54321076u;
constexpr unsigned CODE_SPAN = 5760;
constexpr float NEG_BIG = -3.0e38f;
constexpr int AUX_NT = 2, AUX_SC1 = 16;
constexpr unsigned WS_E = 0, WS_F = 4096, WS_P = WS_F + HKV * NSPLIT * 128, WS_BYTES = WS_P + HKV * NSPLIT * G * D * 4;
constexpr unsigned OOB = 0xF0000000u;
constexpr int REC = D + 4;
constexpr int LDS_TAG = NW * 2 * G * REC;
constexpr unsigned SPIN_CAP = 400000;

typedef unsigned u32x4 __attribute__((ext_vector_type(4)));
typedef unsigned u32x2 __attribute__((ext_vector_type(2)));
typedef float f32x4 __attribute__((ext_vector_type(4)));
typedef float f32x2 __attribute__((ext_vector_type(2)));
typedef __bf16 bf16x2 __attribute__((ext_vector_type(2)));
typedef const __attribute__((address_space(4))) int* cint_p;

__device__ __forceinline__ float dot2(unsigned a, unsigned b, float c) {
  return __builtin_amdgcn_fdot2_f32_bf16(__builtin_bit_cast(bf16x2, a), __builtin_bit_cast(bf16x2, b), c, false);
}
template <int CTRL> __device__ __forceinline__ float dpp(float x) {
  return __builtin_bit_cast(float, __builtin_amdgcn_mov_dpp(__builtin_bit_cast(int, x), CTRL, 0xf, 0xf, true));
}
constexpr int XOR1 = 0xB1, XOR2 = 0x4E, HMIRROR = 0x141, ROR8 = 0x128, ROR4 = 0x124, NEWBCAST = 0x150;

__device__ __forceinline__ float swap16_add(float a, float b) {
  auto s = __builtin_amdgcn_permlane16_swap(__float_as_uint(a), __float_as_uint(b), false, false);
  return __uint_as_float(s[0]) + __uint_as_float(s[1]);
}
__device__ __forceinline__ float xor16_max(float x) {
  auto s = __builtin_amdgcn_permlane16_swap(__float_as_uint(x), __float_as_uint(x), false, false);
  return fmaxf(__uint_as_float(s[0]), __uint_as_float(s[1]));
}
__device__ __forceinline__ float sum32(float x) {
  x += dpp<XOR1>(x); x += dpp<XOR2>(x); x += dpp<HMIRROR>(x); x += dpp<ROR8>(x);
  return swap16_add(x, x);
}
__device__ __forceinline__ float max32(float x) {
  x = fmaxf(x, dpp<XOR1>(x)); x = fmaxf(x, dpp<XOR2>(x)); x = fmaxf(x, dpp<HMIRROR>(x)); x = fmaxf(x, dpp<ROR8>(x));
  return xor16_max(x);
}

#define PIN() do { asm volatile("" ::: "memory"); __builtin_amdgcn_sched_barrier(0); } while (0)

#define TR_STEP(HALF, CTRL, BIT)                                            \
  _Pragma("unroll") for (int u = 0; u < (HALF); ++u) {                      \
    const float lo_ = x[u], hi_ = x[u + (HALF)];                            \
    const float t1_ = lo_ + dpp<CTRL>(lo_), t2_ = hi_ + dpp<CTRL>(hi_);     \
    x[u] = (lane & (BIT)) ? t2_ : t1_;                                      \
  }

struct Tile { u32x4 r[8]; };

__device__ __forceinline__ void issue(Tile& t, __amdgpu_buffer_rsrc_t rs, unsigned voff, unsigned soff) {
#pragma unroll
  for (int j = 0; j < 8; ++j) {
    t.r[j] = __builtin_amdgcn_raw_buffer_load_b128(rs, voff, soff + j * 2 * TOK_BYTES, AUX_NT);
    __builtin_amdgcn_sched_barrier(0);
  }
}

template <int J> __device__ __forceinline__ void pv_step(const u32x4& v, float p, float (&acc)[2][8]) {
  const float p0 = dpp<NEWBCAST + 2 * J>(p), p1 = dpp<NEWBCAST + 2 * J + 1>(p);
#pragma unroll
  for (int k = 0; k < 4; ++k) {
    const float lo = __uint_as_float(v[k] << 16), hi = __uint_as_float(v[k] & 0xffff0000u);
    acc[0][2 * k] = fmaf(p0, lo, acc[0][2 * k]);
    acc[0][2 * k + 1] = fmaf(p0, hi, acc[0][2 * k + 1]);
    acc[1][2 * k] = fmaf(p1, lo, acc[1][2 * k]);
    acc[1][2 * k + 1] = fmaf(p1, hi, acc[1][2 * k + 1]);
  }
  __builtin_amdgcn_sched_barrier(0);
}

__device__ __forceinline__ float rec_weights(const float* rec0, float (&wt)[2 * NW]) {
  f32x2 ml[2 * NW];
#pragma unroll
  for (int r = 0; r < 2 * NW; ++r) ml[r] = *reinterpret_cast<const f32x2*>(rec0 + r * G * REC);
  float M = NEG_BIG;
#pragma unroll
  for (int r = 0; r < 2 * NW; ++r) M = fmaxf(M, ml[r][0]);
  float L = 0.f;
#pragma unroll
  for (int r = 0; r < 2 * NW; ++r) {
    wt[r] = __builtin_amdgcn_exp2f(ml[r][0] - M);
    L = fmaf(wt[r], ml[r][1], L);
  }
  const float inv = L > 0.f ? __builtin_amdgcn_rcpf(L) : 0.f;
#pragma unroll
  for (int r = 0; r < 2 * NW; ++r) wt[r] *= inv;
  return L > 0.f ? M + __builtin_amdgcn_logf(L) : NEG_BIG;
}

__global__ void __launch_bounds__((NW + 1) * 64, 2)
attn_decode(const u32x4* __restrict__ q, const void* __restrict__ kc, const void* __restrict__ vc,
            const int* __restrict__ bt, const int* __restrict__ lens, unsigned char* __restrict__ ws,
            unsigned char* __restrict__ out, int nbps, unsigned kv_bytes) {
  __shared__ __attribute__((aligned(16))) float lds[LDS_TAG + 4];
  unsigned long long t_start = __builtin_amdgcn_s_memrealtime();
  unsigned code_word;
  {
    const unsigned off = min((threadIdx.x & 63) * 128u, CODE_SPAN);
    asm volatile("s_getpc_b64 s[90:91]\n\tglobal_load_dword %0, %1, s[90:91]" : "=&v"(code_word) : "v"(off) : "s90", "s91");
    __builtin_amdgcn_sched_barrier(0);
  }
  asm volatile("" : "+s"(ws), "+s"(out) : "s"(q), "s"(kc), "s"(vc), "s"(bt), "s"(lens), "s"(nbps), "s"(kv_bytes));
  const int head = (HEAD_OF_GROUP >> (4 * (blockIdx.x & 7))) & 7, s = blockIdx.x >> 3;
  const int lane = threadIdx.x & 63;
  const int w = __builtin_amdgcn_readfirstlane(threadIdx.x >> 6);
  const int h = lane >> 5, c = lane & 31;
  const __amdgpu_buffer_rsrc_t wrs = __builtin_amdgcn_make_buffer_rsrc(ws, 0, WS_BYTES, 0x00020000);

  if (__builtin_expect(w < NW, 1)) {
    const __amdgpu_buffer_rsrc_t krs = __builtin_amdgcn_make_buffer_rsrc((void*)kc, 0, kv_bytes, 0x00020000);
    const __amdgpu_buffer_rsrc_t vrs = __builtin_amdgcn_make_buffer_rsrc((void*)vc, 0, kv_bytes, 0x00020000);
    const int wv = w * NSPLIT + s;
    const int bt0 = ((cint_p)bt)[min(wv, nbps - 1)];
    const int len_v = __builtin_nontemporal_load(lens);
    const int btv = bt[min(wv + lane * T, nbps - 1)];
    u32x4 qv[G];
#pragma unroll
    for (int g = 0; g < G; ++g) qv[g] = q[(head * G + g) * (D / 8) + c];
    __builtin_amdgcn_sched_barrier(0);
    const unsigned voff = h * TOK_BYTES + c * 16;
    const unsigned hoff = head * (D * 2);
    Tile K, V;
    {
      const unsigned o0 = (unsigned)bt0 * PAGE_BYTES + hoff;
      issue(K, krs, voff, o0);
      issue(V, vrs, voff, o0);
      PIN();
    }
    const int len_raw = __builtin_amdgcn_readfirstlane(len_v);
    const int skv = nbps * PAGE;
    const bool all_masked = len_raw <= 0;
    const int len = all_masked ? skv : min(len_raw, skv);
    const float sc = all_masked ? 0.f : 0.0625f * 1.4426950408889634f;
    const int npages = (len + PAGE - 1) / PAGE;
    const int n = npages > wv ? (npages - wv + T - 1) / T : 0;
    float m = NEG_BIG, l = 0.f, acc[2][8];
#pragma unroll
    for (int k = 0; k < 8; ++k) { acc[0][k] = 0.f; acc[1][k] = 0.f; }
    int tok = wv * PAGE + 2 * ((lane & 15) >> 1) + h;
    int wr_acc = (((w * 2 + h) * G) * REC + 4 + 8 * c) * 4;
    int wr_ml = ((w * 2 + h) * G + (c & 1)) * REC * 4;
    int rd_ml = (w & 1) * REC * 4, rd_acc = rd_ml + (4 + 128 * (w >> 1) + 2 * lane) * 4;
    int put_p = ((w & 1) * D + 128 * (w >> 1) + 2 * lane) * 4;
    asm volatile("" : "+v"(wr_acc), "+v"(wr_ml), "+v"(rd_ml), "+v"(rd_acc), "+v"(put_p));
    int i = 0;
    if (n > 0) do {
      float x[16];
#pragma unroll
      for (int j = 0; j < 8; ++j)
#pragma unroll
        for (int g = 0; g < G; ++g) {
          float d = 0.f;
#pragma unroll
          for (int k = 0; k < 4; ++k) d = dot2(K.r[j][k], qv[g][k], d);
          x[2 * j + g] = d;
        }
      ++i;
      const unsigned onx = (unsigned)__builtin_amdgcn_readlane(btv, i & 63) * PAGE_BYTES + hoff;
      const unsigned onext = i < n ? onx : OOB;
      PIN();
      issue(K, krs, voff, onext);
      PIN();
      TR_STEP(8, ROR8, 8) TR_STEP(4, HMIRROR, 4) TR_STEP(2, XOR2, 2) TR_STEP(1, XOR1, 1)
      float sv = swap16_add(x[0], x[0]) * sc;
      sv = tok < len ? sv : -INFINITY;
      tok += T * PAGE;
      float pm = fmaxf(sv, dpp<XOR2>(sv));
      pm = fmaxf(pm, dpp<ROR4>(pm));
      pm = fmaxf(pm, dpp<ROR8>(pm));
      const float mn = fmaxf(m, pm);
      if (__builtin_amdgcn_ballot_w64(mn > m)) {
        const float alpha = __builtin_amdgcn_exp2f(m - mn);
        l *= alpha;
        const float a0 = dpp<0x00>(alpha), a1 = dpp<0x55>(alpha);
#pragma unroll
        for (int k = 0; k < 8; ++k) { acc[0][k] *= a0; acc[1][k] *= a1; }
        m = mn;
      }
      const float p = __builtin_amdgcn_exp2f(sv - m);
      l += p;
      pv_step<0>(V.r[0], p, acc); pv_step<1>(V.r[1], p, acc);
      pv_step<2>(V.r[2], p, acc); pv_step<3>(V.r[3], p, acc);
      pv_step<4>(V.r[4], p, acc); pv_step<5>(V.r[5], p, acc);
      pv_step<6>(V.r[6], p, acc); pv_step<7>(V.r[7], p, acc);
      PIN();
      issue(V, vrs, voff, onext);
      PIN();
    } while (i < n);
    l += dpp<XOR2>(l);
    l += dpp<ROR4>(l);
    l += dpp<ROR8>(l);
    char* const lb = reinterpret_cast<char*>(lds);
#pragma unroll
    for (int g = 0; g < G; ++g) {
      *reinterpret_cast<f32x4*>(lb + wr_acc + g * REC * 4) = f32x4{acc[g][0], acc[g][1], acc[g][2], acc[g][3]};
      *reinterpret_cast<f32x4*>(lb + wr_acc + g * REC * 4 + 16) = f32x4{acc[g][4], acc[g][5], acc[g][6], acc[g][7]};
    }
    if (c < 2) *reinterpret_cast<f32x2*>(lb + wr_ml) = f32x2{m, l};
    __syncthreads();
    {
      const unsigned tag = __float_as_uint(lds[LDS_TAG]);
      f32x2 ra[2 * NW];
#pragma unroll
      for (int r = 0; r < 2 * NW; ++r) ra[r] = *reinterpret_cast<const f32x2*>(lb + rd_acc + r * G * REC * 4);
      float wt[2 * NW];
      rec_weights(reinterpret_cast<const float*>(lb + rd_ml), wt);
      f32x2 a = {0.f, 0.f};
#pragma unroll
      for (int r = 0; r < 2 * NW; ++r) a += wt[r] * ra[r];
      const u32x2 ta = {(__float_as_uint(a[0]) & ~0xffu) | (tag & 0xffu),
                        (__float_as_uint(a[1]) & ~0xffu) | ((tag >> 8) & 0xffu)};
      __builtin_amdgcn_raw_buffer_store_b64(ta, wrs, put_p, WS_P + (head * NSPLIT + s) * (G * D * 4), AUX_SC1);
    }
    PIN();
#pragma unroll
    for (int j = 0; j < 8; ++j) asm volatile("" ::"v"(K.r[j]), "v"(V.r[j]));
    asm volatile("" ::"v"(code_word));
    return;
  }

  const __amdgpu_buffer_rsrc_t ors = __builtin_amdgcn_make_buffer_rsrc(out, 0, HKV * G * D * 2, 0x00020000);
  const int g_out = s >> 4;
  const unsigned put_f = c == 0 ? WS_F + (head * NSPLIT + s) * 128 + h * 16 : OOB;
  const unsigned get_f = WS_F + (head * NSPLIT + c) * 128 + g_out * 16;
  const u32x2 e = __builtin_amdgcn_raw_buffer_load_b64(wrs, 0, WS_E + head * 256, AUX_SC1);
  const unsigned long long epoch = (((unsigned long long)e[1] << 32) | e[0]) + 1ull;
  const unsigned ep_lo = (unsigned)epoch, ep_hi = (unsigned)(epoch >> 32);
  const unsigned get = (c == s) ? OOB : WS_P + ((head * NSPLIT + c) * G + g_out) * (D * 4) + (16 * (s & 15) + 8 * h) * 4;
  lds[LDS_TAG] = __uint_as_float(ep_lo);
  asm volatile("" ::"v"(code_word));
  __syncthreads();
  u32x4 f = __builtin_amdgcn_raw_buffer_load_b128(wrs, get_f, 0, AUX_SC1);
  PIN();
  float wt[2 * NW];
  const unsigned ul = __float_as_uint(rec_weights(lds + h * REC, wt));
  __builtin_amdgcn_raw_buffer_store_b128(u32x4{ul, ep_lo, ep_lo, ep_hi}, wrs, put_f, 0, AUX_SC1);
  PIN();
  f32x4 pa = {0.f, 0.f, 0.f, 0.f}, pb = pa;
  {
    const float* rec = lds + g_out * REC + 4 + 16 * (s & 15) + 8 * h;
#pragma unroll
    for (int r = 0; r < 2 * NW; ++r) {
      const float wr = __uint_as_float(__builtin_amdgcn_readlane(__float_as_uint(wt[r]), g_out * 32));
      pa += wr * *reinterpret_cast<const f32x4*>(rec + r * G * REC);
      pb += wr * *reinterpret_cast<const f32x4*>(rec + r * G * REC + 4);
    }
  }
  if (c == s) f = u32x4{(unsigned)__builtin_amdgcn_readlane(ul, g_out * 32), ep_lo, ep_lo, ep_hi};
  auto not_ready = [&]() { return ((f[1] ^ ep_lo) | (f[2] ^ ep_lo) | (f[3] ^ ep_hi)) != 0; };
  bool wait_f = not_ready();
  if (__builtin_amdgcn_ballot_w64(wait_f) != 0) {
    unsigned spin = 0;
#pragma nounroll
    do {
      __builtin_amdgcn_s_sleep(2);
      asm volatile("" ::: "memory");
      if (wait_f) {
        f = __builtin_amdgcn_raw_buffer_load_b128(wrs, get_f, 0, AUX_SC1);
        wait_f = not_ready();
      }
    } while (__builtin_amdgcn_ballot_w64(wait_f) != 0 && ++spin < SPIN_CAP);
  }
  asm volatile("" ::: "memory");
  u32x4 ua = __builtin_amdgcn_raw_buffer_load_b128(wrs, get, 0, AUX_SC1);
  u32x4 ub = __builtin_amdgcn_raw_buffer_load_b128(wrs, get + 16, 0, AUX_SC1);
  __builtin_amdgcn_raw_buffer_store_b64(u32x2{ep_lo, ep_hi}, wrs, (lane == 0 && s == 0) ? WS_E + head * 256 : OOB, 0,
                                        AUX_SC1);
  PIN();
  const float lse = __uint_as_float(f[0]);
  const float M = max32(lse);
  float wgt = __builtin_amdgcn_exp2f(lse - M);
  wgt *= __builtin_amdgcn_rcpf(sum32(wgt));
  PIN();
  const unsigned t0 = ep_lo & 0xffu, t1 = (ep_lo >> 8) & 0xffu;
  auto stale = [&]() {
    const unsigned x0 = (ua[0] ^ t0) | (ua[2] ^ t0) | (ub[0] ^ t0) | (ub[2] ^ t0);
    const unsigned x1 = (ua[1] ^ t1) | (ua[3] ^ t1) | (ub[1] ^ t1) | (ub[3] ^ t1);
    return ((x0 | x1) & 0xffu) != 0 && c != s;
  };
  bool pending = stale();
  if (__builtin_amdgcn_ballot_w64(pending) != 0) {
    unsigned spin = 0;
#pragma nounroll
    do {
      asm volatile("" ::: "memory");
      if (pending) {
        ua = __builtin_amdgcn_raw_buffer_load_b128(wrs, get, 0, AUX_SC1);
        ub = __builtin_amdgcn_raw_buffer_load_b128(wrs, get + 16, 0, AUX_SC1);
        pending = stale();
      }
    } while (__builtin_amdgcn_ballot_w64(pending) != 0 && ++spin < SPIN_CAP);
  }
  float x[8];
#pragma unroll
  for (int k = 0; k < 4; ++k) {
    x[k] = wgt * (c == s ? pa[k] : __uint_as_float(ua[k] & ~0xffu));
    x[4 + k] = wgt * (c == s ? pb[k] : __uint_as_float(ub[k] & ~0xffu));
  }
#pragma unroll
  for (int u = 0; u < 4; ++u) x[u] = swap16_add(x[u], x[u + 4]);
  TR_STEP(2, ROR8, 8) TR_STEP(1, HMIRROR, 4)
  float o = x[0];
  o += dpp<XOR2>(o);
  o += dpp<XOR1>(o);
  const __bf16 ob = (__bf16)o;
  __builtin_amdgcn_raw_buffer_store_b16(__builtin_bit_cast(unsigned short, ob), ors,
                                        (lane & 3) == 0 ? (head * G * D + 16 * s + 8 * h + (c >> 2)) * 2 : OOB, 0, 0);
  {
    const unsigned long long t_end = __builtin_amdgcn_s_memrealtime();
    const unsigned long long extra = 10 * (t_end - t_start);
    while (__builtin_amdgcn_s_memrealtime() - t_end < extra) __builtin_amdgcn_s_sleep(4);
  }
}

}

extern "C" void kernel_launch(void* const* d_in, const int* in_sizes, int n_in, void* d_out, int out_size,
                              void* d_ws, size_t ws_size, hipStream_t stream) {
  const int nbps = in_sizes[3];
  const unsigned kv_bytes = (unsigned)in_sizes[1] * 2u;
  attn_decode<<<HKV * NSPLIT, (NW + 1) * 64, 0, stream>>>((const u32x4*)d_in[0], d_in[1], d_in[2], (const int*)d_in[3],
                                                          (const int*)d_in[4], (unsigned char*)d_ws,
                                                          (unsigned char*)d_out, nbps, kv_bytes);
}
```

```cpp
#define GMODE 0
#define XSEL 0
#include <hip/hip_runtime.h>
#include <hip/hip_bf16.h>
#include <stdint.h>

namespace {

constexpr int D = 256, PAGE = 16, HKV = 8, G = 2, NSPLIT = 32, NW = 4;
constexpr int T = NW * NSPLIT;
constexpr unsigned PAGE_BYTES = PAGE * HKV * D * 2;
constexpr unsigned TOK_BYTES = HKV * D * 2;
constexpr unsigned HEAD_OF_GROUP = 0x54321076u;
constexpr unsigned CODE_SPAN = 5760;
constexpr float NEG_BIG = -3.0e38f;
constexpr int AUX_NT = 2, AUX_SC1 = 16;
constexpr unsigned WS_E = 0, WS_F = 4096, WS_P = WS_F + HKV * NSPLIT * 128, WS_BYTES = WS_P + HKV * NSPLIT * G * D * 4;
constexpr unsigned OOB = 0xF0000000u;
constexpr int REC = D + 4;
constexpr int LDS_TAG = NW * 2 * G * REC;
constexpr unsigned SPIN_CAP = 400000;

typedef unsigned u32x4 __attribute__((ext_vector_type(4)));
typedef unsigned u32x2 __attribute__((ext_vector_type(2)));
typedef float f32x4 __attribute__((ext_vector_type(4)));
typedef float f32x2 __attribute__((ext_vector_type(2)));
typedef __bf16 bf16x2 __attribute__((ext_vector_type(2)));
typedef const __attribute__((address_space(4))) int* cint_p;

__device__ __forceinline__ float dot2(unsigned a, unsigned b, float c) {
  return __builtin_amdgcn_fdot2_f32_bf16(__builtin_bit_cast(bf16x2, a), __builtin_bit_cast(bf16x2, b), c, false);
}
template <int CTRL> __device__ __forceinline__ float dpp(float x) {
  return __builtin_bit_cast(float, __builtin_amdgcn_mov_dpp(__builtin_bit_cast(int, x), CTRL, 0xf, 0xf, true));
}
constexpr int XOR1 = 0xB1, XOR2 = 0x4E, HMIRROR = 0x141, ROR8 = 0x128, ROR4 = 0x124, NEWBCAST = 0x150;

__device__ __forceinline__ float swap16_add(float a, float b) {
  auto s = __builtin_amdgcn_permlane16_swap(__float_as_uint(a), __float_as_uint(b), false, false);
  return __uint_as_float(s[0]) + __uint_as_float(s[1]);
}
__device__ __forceinline__ float xor16_max(float x) {
  auto s = __builtin_amdgcn_permlane16_swap(__float_as_uint(x), __float_as_uint(x), false, false);
  return fmaxf(__uint_as_float(s[0]), __uint_as_float(s[1]));
}
__device__ __forceinline__ float sum32(float x) {
  x += dpp<XOR1>(x); x += dpp<XOR2>(x); x += dpp<HMIRROR>(x); x += dpp<ROR8>(x);
  return swap16_add(x, x);
}
__device__ __forceinline__ float max32(float x) {
  x = fmaxf(x, dpp<XOR1>(x)); x = fmaxf(x, dpp<XOR2>(x)); x = fmaxf(x, dpp<HMIRROR>(x)); x = fmaxf(x, dpp<ROR8>(x));
  return xor16_max(x);
}

#define PIN() do { asm volatile("" ::: "memory"); __builtin_amdgcn_sched_barrier(0); } while (0)

#define TR_STEP(HALF, CTRL, BIT)                                            \
  _Pragma("unroll") for (int u = 0; u < (HALF); ++u) {                      \
    const float lo_ = x[u], hi_ = x[u + (HALF)];                            \
    const float t1_ = lo_ + dpp<CTRL>(lo_), t2_ = hi_ + dpp<CTRL>(hi_);     \
    x[u] = (lane & (BIT)) ? t2_ : t1_;                                      \
  }

struct Tile { u32x4 r[8]; };

__device__ __forceinline__ void issue(Tile& t, __amdgpu_buffer_rsrc_t rs, unsigned voff, unsigned soff) {
#pragma unroll
  for (int j = 0; j < 8; ++j) {
    t.r[j] = __builtin_amdgcn_raw_buffer_load_b128(rs, voff, soff + j * 2 * TOK_BYTES, AUX_NT);
    __builtin_amdgcn_sched_barrier(0);
  }
}

template <int J> __device__ __forceinline__ void pv_step(const u32x4& v, float p, float (&acc)[2][8]) {
  const float p0 = dpp<NEWBCAST + 2 * J>(p), p1 = dpp<NEWBCAST + 2 * J + 1>(p);
#pragma unroll
  for (int k = 0; k < 4; ++k) {
    const float lo = __uint_as_float(v[k] << 16), hi = __uint_as_float(v[k] & 0xffff0000u);
    acc[0][2 * k] = fmaf(p0, lo, acc[0][2 * k]);
    acc[0][2 * k + 1] = fmaf(p0, hi, acc[0][2 * k + 1]);
    acc[1][2 * k] = fmaf(p1, lo, acc[1][2 * k]);
    acc[1][2 * k + 1] = fmaf(p1, hi, acc[1][2 * k + 1]);
  }
  __builtin_amdgcn_sched_barrier(0);
}

__device__ __forceinline__ float rec_weights(const float* rec0, float (&wt)[2 * NW]) {
  f32x2 ml[2 * NW];
#pragma unroll
  for (int r = 0; r < 2 * NW; ++r) ml[r] = *reinterpret_cast<const f32x2*>(rec0 + r * G * REC);
  float M = NEG_BIG;
#pragma unroll
  for (int r = 0; r < 2 * NW; ++r) M = fmaxf(M, ml[r][0]);
  float L = 0.f;
#pragma unroll
  for (int r = 0; r < 2 * NW; ++r) {
    wt[r] = __builtin_amdgcn_exp2f(ml[r][0] - M);
    L = fmaf(wt[r], ml[r][1], L);
  }
  const float inv = L > 0.f ? __builtin_amdgcn_rcpf(L) : 0.f;
#pragma unroll
  for (int r = 0; r < 2 * NW; ++r) wt[r] *= inv;
  return L > 0.f ? M + __builtin_amdgcn_logf(L) : NEG_BIG;
}

__global__ void __launch_bounds__((NW + 1) * 64, 2)
attn_decode(const u32x4* __restrict__ q, const void* __restrict__ kc, const void* __restrict__ vc,
            const int* __restrict__ bt, const int* __restrict__ lens, unsigned char* __restrict__ ws,
            unsigned char* __restrict__ out, int nbps, unsigned kv_bytes) {
  __shared__ __attribute__((aligned(16))) float lds[LDS_TAG + 4];
  const unsigned t_start = (unsigned)__builtin_amdgcn_s_memrealtime();
  unsigned code_word;
  {
    const unsigned off = min((threadIdx.x & 63) * 128u, CODE_SPAN);
    asm volatile("s_getpc_b64 s[90:91]\n\tglobal_load_dword %0, %1, s[90:91]" : "=&v"(code_word) : "v"(off) : "s90", "s91");
    __builtin_amdgcn_sched_barrier(0);
  }
  asm volatile("" : "+s"(ws), "+s"(out) : "s"(q), "s"(kc), "s"(vc), "s"(bt), "s"(lens), "s"(nbps), "s"(kv_bytes));
  const int head = (HEAD_OF_GROUP >> (4 * (blockIdx.x & 7))) & 7, s = blockIdx.x >> 3;
  asm volatile("v_mov_b32 v250, 0" ::: "v250");
  const int lane = threadIdx.x & 63;
  const int w = __builtin_amdgcn_readfirstlane(threadIdx.x >> 6);
  const int h = lane >> 5, c = lane & 31;
  const __amdgpu_buffer_rsrc_t wrs = __builtin_amdgcn_make_buffer_rsrc(ws, 0, WS_BYTES + 8192, 0x00020000);

  if (__builtin_expect(w < NW, 1)) {
    const __amdgpu_buffer_rsrc_t krs = __builtin_amdgcn_make_buffer_rsrc((void*)kc, 0, kv_bytes, 0x00020000);
    const __amdgpu_buffer_rsrc_t vrs = __builtin_amdgcn_make_buffer_rsrc((void*)vc, 0, kv_bytes, 0x00020000);
    const int wv = w * NSPLIT + s;
    const int bt0 = ((cint_p)bt)[min(wv, nbps - 1)];
    const int len_v = __builtin_nontemporal_load(lens);
    const int btv = bt[min(wv + lane * T, nbps - 1)];
    u32x4 qv[G];
#pragma unroll
    for (int g = 0; g < G; ++g) qv[g] = q[(head * G + g) * (D / 8) + c];
    __builtin_amdgcn_sched_barrier(0);
    const unsigned voff = h * TOK_BYTES + c * 16;
    const unsigned hoff = head * (D * 2);
    Tile K, V;
    {
      const unsigned o0 = (unsigned)bt0 * PAGE_BYTES + hoff;
      issue(K, krs, voff, o0);
      issue(V, vrs, voff, o0);
      PIN();
    }
    const int len_raw = __builtin_amdgcn_readfirstlane(len_v);
    const int skv = nbps * PAGE;
    const bool all_masked = len_raw <= 0;
    const int len = all_masked ? skv : min(len_raw, skv);
    const float sc = all_masked ? 0.f : 0.0625f * 1.4426950408889634f;
    const int npages = (len + PAGE - 1) / PAGE;
    const int n = npages > wv ? (npages - wv + T - 1) / T : 0;
    float m = NEG_BIG, l = 0.f, acc[2][8];
#pragma unroll
    for (int k = 0; k < 8; ++k) { acc[0][k] = 0.f; acc[1][k] = 0.f; }
    int tok = wv * PAGE + 2 * ((lane & 15) >> 1) + h;
    int wr_acc = (((w * 2 + h) * G) * REC + 4 + 8 * c) * 4;
    int wr_ml = ((w * 2 + h) * G + (c & 1)) * REC * 4;
    int rd_ml = (w & 1) * REC * 4, rd_acc = rd_ml + (4 + 128 * (w >> 1) + 2 * lane) * 4;
    int put_p = ((w & 1) * D + 128 * (w >> 1) + 2 * lane) * 4;
    asm volatile("" : "+v"(wr_acc), "+v"(wr_ml), "+v"(rd_ml), "+v"(rd_acc), "+v"(put_p));
    int i = 0;
    if (n > 0) do {
      float x[16];
#pragma unroll
      for (int j = 0; j < 8; ++j)
#pragma unroll
        for (int g = 0; g < G; ++g) {
          float d = 0.f;
#pragma unroll
          for (int k = 0; k < 4; ++k) d = dot2(K.r[j][k], qv[g][k], d);
          x[2 * j + g] = d;
        }
      ++i;
      const unsigned onx = (unsigned)__builtin_amdgcn_readlane(btv, i & 63) * PAGE_BYTES + hoff;
      const unsigned onext = i < n ? onx : OOB;
      PIN();
      issue(K, krs, voff, onext);
      PIN();
      TR_STEP(8, ROR8, 8) TR_STEP(4, HMIRROR, 4) TR_STEP(2, XOR2, 2) TR_STEP(1, XOR1, 1)
      float sv = swap16_add(x[0], x[0]) * sc;
      sv = tok < len ? sv : -INFINITY;
      tok += T * PAGE;
      float pm = fmaxf(sv, dpp<XOR2>(sv));
      pm = fmaxf(pm, dpp<ROR4>(pm));
      pm = fmaxf(pm, dpp<ROR8>(pm));
      const float mn = fmaxf(m, pm);
      if (__builtin_amdgcn_ballot_w64(mn > m)) {
        const float alpha = __builtin_amdgcn_exp2f(m - mn);
        l *= alpha;
        const float a0 = dpp<0x00>(alpha), a1 = dpp<0x55>(alpha);
#pragma unroll
        for (int k = 0; k < 8; ++k) { acc[0][k] *= a0; acc[1][k] *= a1; }
        m = mn;
      }
      const float p = __builtin_amdgcn_exp2f(sv - m);
      l += p;
      pv_step<0>(V.r[0], p, acc); pv_step<1>(V.r[1], p, acc);
      pv_step<2>(V.r[2], p, acc); pv_step<3>(V.r[3], p, acc);
      pv_step<4>(V.r[4], p, acc); pv_step<5>(V.r[5], p, acc);
      pv_step<6>(V.r[6], p, acc); pv_step<7>(V.r[7], p, acc);
      PIN();
      issue(V, vrs, voff, onext);
      PIN();
    } while (i < n);
    l += dpp<XOR2>(l);
    l += dpp<ROR4>(l);
    l += dpp<ROR8>(l);
    char* const lb = reinterpret_cast<char*>(lds);
#pragma unroll
    for (int g = 0; g < G; ++g) {
      *reinterpret_cast<f32x4*>(lb + wr_acc + g * REC * 4) = f32x4{acc[g][0], acc[g][1], acc[g][2], acc[g][3]};
      *reinterpret_cast<f32x4*>(lb + wr_acc + g * REC * 4 + 16) = f32x4{acc[g][4], acc[g][5], acc[g][6], acc[g][7]};
    }
    if (c < 2) *reinterpret_cast<f32x2*>(lb + wr_ml) = f32x2{m, l};
    __syncthreads();
    {
      const unsigned tag = __float_as_uint(lds[LDS_TAG]);
      f32x2 ra[2 * NW];
#pragma unroll
      for (int r = 0; r < 2 * NW; ++r) ra[r] = *reinterpret_cast<const f32x2*>(lb + rd_acc + r * G * REC * 4);
      float wt[2 * NW];
      rec_weights(reinterpret_cast<const float*>(lb + rd_ml), wt);
      f32x2 a = {0.f, 0.f};
#pragma unroll
      for (int r = 0; r < 2 * NW; ++r) a += wt[r] * ra[r];
      const u32x2 ta = {(__float_as_uint(a[0]) & ~0xffu) | (tag & 0xffu),
                        (__float_as_uint(a[1]) & ~0xffu) | ((tag >> 8) & 0xffu)};
      __builtin_amdgcn_raw_buffer_store_b64(ta, wrs, put_p, WS_P + (head * NSPLIT + s) * (G * D * 4), AUX_SC1);
    }
    PIN();
#pragma unroll
    for (int j = 0; j < 8; ++j) asm volatile("" ::"v"(K.r[j]), "v"(V.r[j]));
    asm volatile("" ::"v"(code_word));
    return;
  }

  const __amdgpu_buffer_rsrc_t ors = __builtin_amdgcn_make_buffer_rsrc(out, 0, HKV * G * D * 2, 0x00020000);
  const int g_out = s >> 4;
  const unsigned put_f = c == 0 ? WS_F + (head * NSPLIT + s) * 128 + h * 16 : OOB;
  const unsigned get_f = WS_F + (head * NSPLIT + c) * 128 + g_out * 16;
  const u32x2 e = __builtin_amdgcn_raw_buffer_load_b64(wrs, 0, WS_E + head * 256, AUX_SC1);
  const unsigned long long epoch = (((unsigned long long)e[1] << 32) | e[0]) + 1ull;
  const unsigned ep_lo = (unsigned)epoch, ep_hi = (unsigned)(epoch >> 32);
  const unsigned get = (c == s) ? OOB : WS_P + ((head * NSPLIT + c) * G + g_out) * (D * 4) + (16 * (s & 15) + 8 * h) * 4;
  lds[LDS_TAG] = __uint_as_float(ep_lo);
  asm volatile("" ::"v"(code_word));
  __syncthreads();
  u32x4 f = __builtin_amdgcn_raw_buffer_load_b128(wrs, get_f, 0, AUX_SC1);
  PIN();
  float wt[2 * NW];
  const unsigned ul = __float_as_uint(rec_weights(lds + h * REC, wt));
  __builtin_amdgcn_raw_buffer_store_b128(u32x4{ul, ep_lo, ep_lo, ep_hi}, wrs, put_f, 0, AUX_SC1);
  PIN();
  f32x4 pa = {0.f, 0.f, 0.f, 0.f}, pb = pa;
  {
    const float* rec = lds + g_out * REC + 4 + 16 * (s & 15) + 8 * h;
#pragma unroll
    for (int r = 0; r < 2 * NW; ++r) {
      const float wr = __uint_as_float(__builtin_amdgcn_readlane(__float_as_uint(wt[r]), g_out * 32));
      pa += wr * *reinterpret_cast<const f32x4*>(rec + r * G * REC);
      pb += wr * *reinterpret_cast<const f32x4*>(rec + r * G * REC + 4);
    }
  }
  if (c == s) f = u32x4{(unsigned)__builtin_amdgcn_readlane(ul, g_out * 32), ep_lo, ep_lo, ep_hi};
  auto not_ready = [&]() { return ((f[1] ^ ep_lo) | (f[2] ^ ep_lo) | (f[3] ^ ep_hi)) != 0; };
  bool wait_f = not_ready();
  if (__builtin_amdgcn_ballot_w64(wait_f) != 0) {
    unsigned spin = 0;
#pragma nounroll
    do {
      __builtin_amdgcn_s_sleep(2);
      asm volatile("" ::: "memory");
      if (wait_f) {
        f = __builtin_amdgcn_raw_buffer_load_b128(wrs, get_f, 0, AUX_SC1);
        wait_f = not_ready();
      }
    } while (__builtin_amdgcn_ballot_w64(wait_f) != 0 && ++spin < SPIN_CAP);
  }
  asm volatile("" ::: "memory");
  u32x4 ua = __builtin_amdgcn_raw_buffer_load_b128(wrs, get, 0, AUX_SC1);
  u32x4 ub = __builtin_amdgcn_raw_buffer_load_b128(wrs, get + 16, 0, AUX_SC1);
  __builtin_amdgcn_raw_buffer_store_b64(u32x2{ep_lo, ep_hi}, wrs, (lane == 0 && s == 0) ? WS_E + head * 256 : OOB, 0,
                                        AUX_SC1);
  PIN();
  const float lse = __uint_as_float(f[0]);
  const float M = max32(lse);
  float wgt = __builtin_amdgcn_exp2f(lse - M);
  wgt *= __builtin_amdgcn_rcpf(sum32(wgt));
  PIN();
  const unsigned t0 = ep_lo & 0xffu, t1 = (ep_lo >> 8) & 0xffu;
  auto stale = [&]() {
    const unsigned x0 = (ua[0] ^ t0) | (ua[2] ^ t0) | (ub[0] ^ t0) | (ub[2] ^ t0);
    const unsigned x1 = (ua[1] ^ t1) | (ua[3] ^ t1) | (ub[1] ^ t1) | (ub[3] ^ t1);
    return ((x0 | x1) & 0xffu) != 0 && c != s;
  };
  bool pending = stale();
  if (__builtin_amdgcn_ballot_w64(pending) != 0) {
    unsigned spin = 0;
#pragma nounroll
    do {
      asm volatile("" ::: "memory");
      if (pending) {
        ua = __builtin_amdgcn_raw_buffer_load_b128(wrs, get, 0, AUX_SC1);
        ub = __builtin_amdgcn_raw_buffer_load_b128(wrs, get + 16, 0, AUX_SC1);
        pending = stale();
      }
    } while (__builtin_amdgcn_ballot_w64(pending) != 0 && ++spin < SPIN_CAP);
  }
  float x[8];
#pragma unroll
  for (int k = 0; k < 4; ++k) {
    x[k] = wgt * (c == s ? pa[k] : __uint_as_float(ua[k] & ~0xffu));
    x[4 + k] = wgt * (c == s ? pb[k] : __uint_as_float(ub[k] & ~0xffu));
  }
#pragma unroll
  for (int u = 0; u < 4; ++u) x[u] = swap16_add(x[u], x[u + 4]);
  TR_STEP(2, ROR8, 8) TR_STEP(1, HMIRROR, 4)
  float o = x[0];
  o += dpp<XOR2>(o);
  o += dpp<XOR1>(o);
  const __bf16 ob = (__bf16)o;
  __builtin_amdgcn_raw_buffer_store_b16(__builtin_bit_cast(unsigned short, ob), ors,
                                        (lane & 3) == 0 ? (head * G * D + 16 * s + 8 * h + (c >> 2)) * 2 : OOB, 0, 0);
  {
    const unsigned t_end = (unsigned)__builtin_amdgcn_s_memrealtime();
    __builtin_amdgcn_raw_buffer_store_b64(u32x2{t_start, t_end}, wrs, lane == 0 ? WS_BYTES + blockIdx.x * 8 : OOB, 0, AUX_SC1);
    if (blockIdx.x == 0) {
      while ((unsigned)__builtin_amdgcn_s_memrealtime() - t_start < 8000u) __builtin_amdgcn_s_sleep(4);
      asm volatile("" ::: "memory");
      const u32x4 a = __builtin_amdgcn_raw_buffer_load_b128(wrs, WS_BYTES + lane * 32, 0, AUX_SC1);
      const u32x4 b = __builtin_amdgcn_raw_buffer_load_b128(wrs, WS_BYTES + lane * 32 + 16, 0, AUX_SC1);
      int st[4] = {(int)(a[0] - t_start), (int)(a[2] - t_start), (int)(b[0] - t_start), (int)(b[2] - t_start)};
      int en[4] = {(int)(a[1] - t_start), (int)(a[3] - t_start), (int)(b[1] - t_start), (int)(b[3] - t_start)};
      int mn = 1 << 30, mx = -(1 << 30), emx = -(1 << 30), xs = 1 << 30;
      for (int u = 0; u < 4; ++u) { mn = min(mn, st[u]); mx = max(mx, st[u]); emx = max(emx, en[u]); if (((4 * lane + u) & 7) == XSEL) xs = min(xs, st[u]); }
      for (int o = 1; o < 64; o <<= 1) { mn = min(mn, __shfl_xor(mn, o)); mx = max(mx, __shfl_xor(mx, o)); emx = max(emx, __shfl_xor(emx, o)); xs = min(xs, __shfl_xor(xs, o)); }
      const unsigned extra = GMODE == 1 ? 50u * (unsigned)(mx - mn) : GMODE == 2 ? 10u * (unsigned)(emx - mn) : GMODE == 3 ? 50u * (unsigned)(xs - mn) : GMODE == 4 ? 20u * (unsigned)(8000 - emx) : 0u;
      while ((unsigned)__builtin_amdgcn_s_memrealtime() - t_start < 8200u + extra) __builtin_amdgcn_s_sleep(4);
    }
  }
}

}

extern "C" void kernel_launch(void* const* d_in, const int* in_sizes, int n_in, void* d_out, int out_size,
                              void* d_ws, size_t ws_size, hipStream_t stream) {
  const int nbps = in_sizes[3];
  const unsigned kv_bytes = (unsigned)in_sizes[1] * 2u;
  attn_decode<<<HKV * NSPLIT, (NW + 1) * 64, 0, stream>>>((const u32x4*)d_in[0], d_in[1], d_in[2], (const int*)d_in[3],
                                                          (const int*)d_in[4], (unsigned char*)d_ws,
                                                          (unsigned char*)d_out, nbps, kv_bytes);
}
```

```cpp
#include <hip/hip_runtime.h>
#include <hip/hip_bf16.h>
#include <stdint.h>

namespace {

constexpr int D = 256, PAGE = 16, HKV = 8, G = 2, NSPLIT = 32, NW = 4;
constexpr int T = NW * NSPLIT;
constexpr unsigned PAGE_BYTES = PAGE * HKV * D * 2;
constexpr unsigned TOK_BYTES = HKV * D * 2;
constexpr unsigned HEAD_OF_GROUP = 0x54321076u;
constexpr unsigned CODE_SPAN = 5760;
constexpr float NEG_BIG = -3.0e38f;
constexpr int AUX_NT = 2, AUX_SC1 = 16;
constexpr unsigned WS_E = 0, WS_F = 4096, WS_P = WS_F + HKV * NSPLIT * 128, WS_BYTES = WS_P + HKV * NSPLIT * G * D * 4;
constexpr unsigned OOB = 0xF0000000u;
constexpr int REC = D + 4;
constexpr int LDS_TAG = NW * 2 * G * REC;
constexpr unsigned SPIN_CAP = 400000;

typedef unsigned u32x4 __attribute__((ext_vector_type(4)));
typedef unsigned u32x2 __attribute__((ext_vector_type(2)));
typedef float f32x4 __attribute__((ext_vector_type(4)));
typedef float f32x2 __attribute__((ext_vector_type(2)));
typedef __bf16 bf16x2 __attribute__((ext_vector_type(2)));
typedef const __attribute__((address_space(4))) int* cint_p;

__device__ __forceinline__ float dot2(unsigned a, unsigned b, float c) {
  return __builtin_amdgcn_fdot2_f32_bf16(__builtin_bit_cast(bf16x2, a), __builtin_bit_cast(bf16x2, b), c, false);
}
template <int CTRL> __device__ __forceinline__ float dpp(float x) {
  return __builtin_bit_cast(float, __builtin_amdgcn_mov_dpp(__builtin_bit_cast(int, x), CTRL, 0xf, 0xf, true));
}
constexpr int XOR1 = 0xB1, XOR2 = 0x4E, HMIRROR = 0x141, ROR8 = 0x128, ROR4 = 0x124, NEWBCAST = 0x150;

__device__ __forceinline__ float swap16_add(float a, float b) {
  auto s = __builtin_amdgcn_permlane16_swap(__float_as_uint(a), __float_as_uint(b), false, false);
  return __uint_as_float(s[0]) + __uint_as_float(s[1]);
}
__device__ __forceinline__ float xor16_max(float x) {
  auto s = __builtin_amdgcn_permlane16_swap(__float_as_uint(x), __float_as_uint(x), false, false);
  return fmaxf(__uint_as_float(s[0]), __uint_as_float(s[1]));
}
__device__ __forceinline__ float sum32(float x) {
  x += dpp<XOR1>(x); x += dpp<XOR2>(x); x += dpp<HMIRROR>(x); x += dpp<ROR8>(x);
  return swap16_add(x, x);
}
__device__ __forceinline__ float max32(float x) {
  x = fmaxf(x, dpp<XOR1>(x)); x = fmaxf(x, dpp<XOR2>(x)); x = fmaxf(x, dpp<HMIRROR>(x)); x = fmaxf(x, dpp<ROR8>(x));
  return xor16_max(x);
}

#define PIN() do { asm volatile("" ::: "memory"); __builtin_amdgcn_sched_barrier(0); } while (0)

#define TR_STEP(HALF, CTRL, BIT)                                            \
  _Pragma("unroll") for (int u = 0; u < (HALF); ++u) {                      \
    const float lo_ = x[u], hi_ = x[u + (HALF)];                            \
    const float t1_ = lo_ + dpp<CTRL>(lo_), t2_ = hi_ + dpp<CTRL>(hi_);     \
    x[u] = (lane & (BIT)) ? t2_ : t1_;                                      \
  }

struct Tile { u32x4 r[8]; };

__device__ __forceinline__ void issue(Tile& t, __amdgpu_buffer_rsrc_t rs, unsigned voff, unsigned soff) {
#pragma unroll
  for (int j = 0; j < 8; ++j) {
    t.r[j] = __builtin_amdgcn_raw_buffer_load_b128(rs, voff, soff + j * 2 * TOK_BYTES, AUX_NT);
    __builtin_amdgcn_sched_barrier(0);
  }
}

template <int J> __device__ __forceinline__ void pv_step(const u32x4& v, float p, float (&acc)[2][8]) {
  const float p0 = dpp<NEWBCAST + 2 * J>(p), p1 = dpp<NEWBCAST + 2 * J + 1>(p);
#pragma unroll
  for (int k = 0; k < 4; ++k) {
    const float lo = __uint_as_float(v[k] << 16), hi = __uint_as_float(v[k] & 0xffff0000u);
    acc[0][2 * k] = fmaf(p0, lo, acc[0][2 * k]);
    acc[0][2 * k + 1] = fmaf(p0, hi, acc[0][2 * k + 1]);
    acc[1][2 * k] = fmaf(p1, lo, acc[1][2 * k]);
    acc[1][2 * k + 1] = fmaf(p1, hi, acc[1][2 * k + 1]);
  }
  __builtin_amdgcn_sched_barrier(0);
}

__device__ __forceinline__ float rec_weights(const float* rec0, float (&wt)[2 * NW]) {
  f32x2 ml[2 * NW];
#pragma unroll
  for (int r = 0; r < 2 * NW; ++r) ml[r] = *reinterpret_cast<const f32x2*>(rec0 + r * G * REC);
  float M = NEG_BIG;
#pragma unroll
  for (int r = 0; r < 2 * NW; ++r) M = fmaxf(M, ml[r][0]);
  float L = 0.f;
#pragma unroll
  for (int r = 0; r < 2 * NW; ++r) {
    wt[r] = __builtin_amdgcn_exp2f(ml[r][0] - M);
    L = fmaf(wt[r], ml[r][1], L);
  }
  const float inv = L > 0.f ? __builtin_amdgcn_rcpf(L) : 0.f;
#pragma unroll
  for (int r = 0; r < 2 * NW; ++r) wt[r] *= inv;
  return L > 0.f ? M + __builtin_amdgcn_logf(L) : NEG_BIG;
}

__global__ void __launch_bounds__((NW + 1) * 64, 2)
attn_decode(const u32x4* __restrict__ q, const void* __restrict__ kc, const void* __restrict__ vc,
            const int* __restrict__ bt, const int* __restrict__ lens, unsigned char* __restrict__ ws,
            unsigned char* __restrict__ out, int nbps, unsigned kv_bytes) {
  __shared__ __attribute__((aligned(16))) float lds[LDS_TAG + 4];
  const unsigned g2_start = (unsigned)__builtin_amdgcn_s_memrealtime();
  unsigned code_word;
  {
    const unsigned off = min((threadIdx.x & 63) * 128u, CODE_SPAN);
    asm volatile("s_getpc_b64 s[90:91]\n\tglobal_load_dword %0, %1, s[90:91]" : "=&v"(code_word) : "v"(off) : "s90", "s91");
    __builtin_amdgcn_sched_barrier(0);
  }
  asm volatile("" : "+s"(ws), "+s"(out) : "s"(q), "s"(kc), "s"(vc), "s"(bt), "s"(lens), "s"(nbps), "s"(kv_bytes));
  const int head = (HEAD_OF_GROUP >> (4 * (blockIdx.x & 7))) & 7, s = blockIdx.x >> 3;
  const int lane = threadIdx.x & 63;
  const int w = __builtin_amdgcn_readfirstlane(threadIdx.x >> 6);
  const int h = lane >> 5, c = lane & 31;
  const __amdgpu_buffer_rsrc_t wrs = __builtin_amdgcn_make_buffer_rsrc(ws, 0, WS_BYTES, 0x00020000);

  if (__builtin_expect(w < NW, 1)) {
    const __amdgpu_buffer_rsrc_t krs = __builtin_amdgcn_make_buffer_rsrc((void*)kc, 0, kv_bytes, 0x00020000);
    const __amdgpu_buffer_rsrc_t vrs = __builtin_amdgcn_make_buffer_rsrc((void*)vc, 0, kv_bytes, 0x00020000);
    const int wv = w * NSPLIT + s;
    const int bt0 = ((cint_p)bt)[min(wv, nbps - 1)];
    const int len_v = __builtin_nontemporal_load(lens);
    const int btv = bt[min(wv + lane * T, nbps - 1)];
    u32x4 qv[G];
#pragma unroll
    for (int g = 0; g < G; ++g) qv[g] = q[(head * G + g) * (D / 8) + c];
    __builtin_amdgcn_sched_barrier(0);
    const unsigned voff = h * TOK_BYTES + c * 16;
    const unsigned hoff = head * (D * 2);
    Tile K, V;
    {
      const unsigned o0 = (unsigned)bt0 * PAGE_BYTES + hoff;
      issue(K, krs, voff, o0);
      issue(V, vrs, voff, o0);
      PIN();
    }
    const int len_raw = __builtin_amdgcn_readfirstlane(len_v);
    const int skv = nbps * PAGE;
    const bool all_masked = len_raw <= 0;
    const int len = all_masked ? skv : min(len_raw, skv);
    const float sc = all_masked ? 0.f : 0.0625f * 1.4426950408889634f;
    const int npages = (len + PAGE - 1) / PAGE;
    const int n = npages > wv ? (npages - wv + T - 1) / T : 0;
    float m = NEG_BIG, l = 0.f, acc[2][8];
#pragma unroll
    for (int k = 0; k < 8; ++k) { acc[0][k] = 0.f; acc[1][k] = 0.f; }
    int tok = wv * PAGE + 2 * ((lane & 15) >> 1) + h;
    int wr_acc = (((w * 2 + h) * G) * REC + 4 + 8 * c) * 4;
    int wr_ml = ((w * 2 + h) * G + (c & 1)) * REC * 4;
    int rd_ml = (w & 1) * REC * 4, rd_acc = rd_ml + (4 + 128 * (w >> 1) + 2 * lane) * 4;
    int put_p = ((w & 1) * D + 128 * (w >> 1) + 2 * lane) * 4;
    asm volatile("" : "+v"(wr_acc), "+v"(wr_ml), "+v"(rd_ml), "+v"(rd_acc), "+v"(put_p));
    int i = 0;
    if (n > 0) do {
      float x[16];
#pragma unroll
      for (int j = 0; j < 8; ++j)
#pragma unroll
        for (int g = 0; g < G; ++g) {
          float d = 0.f;
#pragma unroll
          for (int k = 0; k < 4; ++k) d = dot2(K.r[j][k], qv[g][k], d);
          x[2 * j + g] = d;
        }
      ++i;
      const unsigned onx = (unsigned)__builtin_amdgcn_readlane(btv, i & 63) * PAGE_BYTES + hoff;
      const unsigned onext = i < n ? onx : OOB;
      PIN();
      issue(K, krs, voff, onext);
      PIN();
      TR_STEP(8, ROR8, 8) TR_STEP(4, HMIRROR, 4) TR_STEP(2, XOR2, 2) TR_STEP(1, XOR1, 1)
      float sv = swap16_add(x[0], x[0]) * sc;
      sv = tok < len ? sv : -INFINITY;
      tok += T * PAGE;
      float pm = fmaxf(sv, dpp<XOR2>(sv));
      pm = fmaxf(pm, dpp<ROR4>(pm));
      pm = fmaxf(pm, dpp<ROR8>(pm));
      const float mn = fmaxf(m, pm);
      if (__builtin_amdgcn_ballot_w64(mn > m)) {
        const float alpha = __builtin_amdgcn_exp2f(m - mn);
        l *= alpha;
        const float a0 = dpp<0x00>(alpha), a1 = dpp<0x55>(alpha);
#pragma unroll
        for (int k = 0; k < 8; ++k) { acc[0][k] *= a0; acc[1][k] *= a1; }
        m = mn;
      }
      const float p = __builtin_amdgcn_exp2f(sv - m);
      l += p;
      pv_step<0>(V.r[0], p, acc); pv_step<1>(V.r[1], p, acc);
      pv_step<2>(V.r[2], p, acc); pv_step<3>(V.r[3], p, acc);
      pv_step<4>(V.r[4], p, acc); pv_step<5>(V.r[5], p, acc);
      pv_step<6>(V.r[6], p, acc); pv_step<7>(V.r[7], p, acc);
      PIN();
      issue(V, vrs, voff, onext);
      PIN();
    } while (i < n);
    l += dpp<XOR2>(l);
    l += dpp<ROR4>(l);
    l += dpp<ROR8>(l);
    char* const lb = reinterpret_cast<char*>(lds);
#pragma unroll
    for (int g = 0; g < G; ++g) {
      *reinterpret_cast<f32x4*>(lb + wr_acc + g * REC * 4) = f32x4{acc[g][0], acc[g][1], acc[g][2], acc[g][3]};
      *reinterpret_cast<f32x4*>(lb + wr_acc + g * REC * 4 + 16) = f32x4{acc[g][4], acc[g][5], acc[g][6], acc[g][7]};
    }
    if (c < 2) *reinterpret_cast<f32x2*>(lb + wr_ml) = f32x2{m, l};
    __syncthreads();
    {
      const unsigned tag = __float_as_uint(lds[LDS_TAG]);
      f32x2 ra[2 * NW];
#pragma unroll
      for (int r = 0; r < 2 * NW; ++r) ra[r] = *reinterpret_cast<const f32x2*>(lb + rd_acc + r * G * REC * 4);
      float wt[2 * NW];
      rec_weights(reinterpret_cast<const float*>(lb + rd_ml), wt);
      f32x2 a = {0.f, 0.f};
#pragma unroll
      for (int r = 0; r < 2 * NW; ++r) a += wt[r] * ra[r];
      const u32x2 ta = {(__float_as_uint(a[0]) & ~0xffu) | (tag & 0xffu),
                        (__float_as_uint(a[1]) & ~0xffu) | ((tag >> 8) & 0xffu)};
      __builtin_amdgcn_raw_buffer_store_b64(ta, wrs, put_p, WS_P + (head * NSPLIT + s) * (G * D * 4), AUX_SC1);
    }
    PIN();
#pragma unroll
    for (int j = 0; j < 8; ++j) asm volatile("" ::"v"(K.r[j]), "v"(V.r[j]));
    asm volatile("" ::"v"(code_word));
    return;
  }

  const __amdgpu_buffer_rsrc_t ors = __builtin_amdgcn_make_buffer_rsrc(out, 0, HKV * G * D * 2, 0x00020000);
  const int g_out = s >> 4;
  const unsigned put_f = c == 0 ? WS_F + (head * NSPLIT + s) * 128 + h * 16 : OOB;
  const unsigned get_f = WS_F + (head * NSPLIT + c) * 128 + g_out * 16;
  const u32x2 e = __builtin_amdgcn_raw_buffer_load_b64(wrs, 0, WS_E + head * 256, AUX_SC1);
  const unsigned long long epoch = (((unsigned long long)e[1] << 32) | e[0]) + 1ull;
  const unsigned ep_lo = (unsigned)epoch, ep_hi = (unsigned)(epoch >> 32);
  const unsigned get = (c == s) ? OOB : WS_P + ((head * NSPLIT + c) * G + g_out) * (D * 4) + (16 * (s & 15) + 8 * h) * 4;
  lds[LDS_TAG] = __uint_as_float(ep_lo);
  asm volatile("" ::"v"(code_word));
  __syncthreads();
  u32x4 f = __builtin_amdgcn_raw_buffer_load_b128(wrs, get_f, 0, AUX_SC1);
  PIN();
  float wt[2 * NW];
  const unsigned ul = __float_as_uint(rec_weights(lds + h * REC, wt));
  __builtin_amdgcn_raw_buffer_store_b128(u32x4{ul, ep_lo, ep_lo, ep_hi}, wrs, put_f, 0, AUX_SC1);
  PIN();
  f32x4 pa = {0.f, 0.f, 0.f, 0.f}, pb = pa;
  {
    const float* rec = lds + g_out * REC + 4 + 16 * (s & 15) + 8 * h;
#pragma unroll
    for (int r = 0; r < 2 * NW; ++r) {
      const float wr = __uint_as_float(__builtin_amdgcn_readlane(__float_as_uint(wt[r]), g_out * 32));
      pa += wr * *reinterpret_cast<const f32x4*>(rec + r * G * REC);
      pb += wr * *reinterpret_cast<const f32x4*>(rec + r * G * REC + 4);
    }
  }
  if (c == s) f = u32x4{(unsigned)__builtin_amdgcn_readlane(ul, g_out * 32), ep_lo, ep_lo, ep_hi};
  auto not_ready = [&]() { return ((f[1] ^ ep_lo) | (f[2] ^ ep_lo) | (f[3] ^ ep_hi)) != 0; };
  bool wait_f = not_ready();
  if (__builtin_amdgcn_ballot_w64(wait_f) != 0) {
    unsigned spin = 0;
#pragma nounroll
    do {
      __builtin_amdgcn_s_sleep(2);
      asm volatile("" ::: "memory");
      if (wait_f) {
        f = __builtin_amdgcn_raw_buffer_load_b128(wrs, get_f, 0, AUX_SC1);
        wait_f = not_ready();
      }
    } while (__builtin_amdgcn_ballot_w64(wait_f) != 0 && ++spin < SPIN_CAP);
  }
  asm volatile("" ::: "memory");
  u32x4 ua = __builtin_amdgcn_raw_buffer_load_b128(wrs, get, 0, AUX_SC1);
  u32x4 ub = __builtin_amdgcn_raw_buffer_load_b128(wrs, get + 16, 0, AUX_SC1);
  __builtin_amdgcn_raw_buffer_store_b64(u32x2{ep_lo, ep_hi}, wrs, (lane == 0 && s == 0) ? WS_E + head * 256 : OOB, 0,
                                        AUX_SC1);
  PIN();
  const float lse = __uint_as_float(f[0]);
  const float M = max32(lse);
  float wgt = __builtin_amdgcn_exp2f(lse - M);
  wgt *= __builtin_amdgcn_rcpf(sum32(wgt));
  PIN();
  const unsigned t0 = ep_lo & 0xffu, t1 = (ep_lo >> 8) & 0xffu;
  auto stale = [&]() {
    const unsigned x0 = (ua[0] ^ t0) | (ua[2] ^ t0) | (ub[0] ^ t0) | (ub[2] ^ t0);
    const unsigned x1 = (ua[1] ^ t1) | (ua[3] ^ t1) | (ub[1] ^ t1) | (ub[3] ^ t1);
    return ((x0 | x1) & 0xffu) != 0 && c != s;
  };
  bool pending = stale();
  if (__builtin_amdgcn_ballot_w64(pending) != 0) {
    unsigned spin = 0;
#pragma nounroll
    do {
      asm volatile("" ::: "memory");
      if (pending) {
        ua = __builtin_amdgcn_raw_buffer_load_b128(wrs, get, 0, AUX_SC1);
        ub = __builtin_amdgcn_raw_buffer_load_b128(wrs, get + 16, 0, AUX_SC1);
        pending = stale();
      }
    } while (__builtin_amdgcn_ballot_w64(pending) != 0 && ++spin < SPIN_CAP);
  }
  float x[8];
#pragma unroll
  for (int k = 0; k < 4; ++k) {
    x[k] = wgt * (c == s ? pa[k] : __uint_as_float(ua[k] & ~0xffu));
    x[4 + k] = wgt * (c == s ? pb[k] : __uint_as_float(ub[k] & ~0xffu));
  }
#pragma unroll
  for (int u = 0; u < 4; ++u) x[u] = swap16_add(x[u], x[u + 4]);
  TR_STEP(2, ROR8, 8) TR_STEP(1, HMIRROR, 4)
  float o = x[0];
  o += dpp<XOR2>(o);
  o += dpp<XOR1>(o);
  const __bf16 ob = (__bf16)o;
  __builtin_amdgcn_raw_buffer_store_b16(__builtin_bit_cast(unsigned short, ob), ors,
                                        (lane & 3) == 0 ? (head * G * D + 16 * s + 8 * h + (c >> 2)) * 2 : OOB, 0, 0);
  {
    const unsigned t_end = (unsigned)__builtin_amdgcn_s_memrealtime();
    const __amdgpu_buffer_rsrc_t drs = __builtin_amdgcn_make_buffer_rsrc(ws + (4u << 20), 0, 8192, 0x00020000);
    __builtin_amdgcn_raw_buffer_store_b64(u32x2{g2_start, t_end}, drs, lane == 0 ? blockIdx.x * 8 : 0xF0000000u, 0, 16);
    if (blockIdx.x == 0) {
      while ((unsigned)__builtin_amdgcn_s_memrealtime() - g2_start < 8000u) __builtin_amdgcn_s_sleep(4);
      asm volatile("" ::: "memory");
      const u32x4 a = __builtin_amdgcn_raw_buffer_load_b128(drs, lane * 32, 0, 16);
      const u32x4 b = __builtin_amdgcn_raw_buffer_load_b128(drs, lane * 32 + 16, 0, 16);
      int st[4] = {(int)(a[0] - g2_start), (int)(a[2] - g2_start), (int)(b[0] - g2_start), (int)(b[2] - g2_start)};
      int en[4] = {(int)(a[1] - g2_start), (int)(a[3] - g2_start), (int)(b[1] - g2_start), (int)(b[3] - g2_start)};
      int mn = 1 << 30, emx = -(1 << 30);
      for (int u = 0; u < 4; ++u) { mn = min(mn, st[u]); emx = max(emx, en[u]); }
      for (int o = 1; o < 64; o <<= 1) { mn = min(mn, __shfl_xor(mn, o)); emx = max(emx, __shfl_xor(emx, o)); }
      const unsigned extra = 10u * (unsigned)(emx - mn);
      while ((unsigned)__builtin_amdgcn_s_memrealtime() - g2_start < 8200u + extra) __builtin_amdgcn_s_sleep(4);
    }
  }
}

}

extern "C" void kernel_launch(void* const* d_in, const int* in_sizes, int n_in, void* d_out, int out_size,
                              void* d_ws, size_t ws_size, hipStream_t stream) {
  const int nbps = in_sizes[3];
  const unsigned kv_bytes = (unsigned)in_sizes[1] * 2u;
  attn_decode<<<HKV * NSPLIT, (NW + 1) * 64, 0, stream>>>((const u32x4*)d_in[0], d_in[1], d_in[2], (const int*)d_in[3],
                                                          (const int*)d_in[4], (unsigned char*)d_ws,
                                                          (unsigned char*)d_out, nbps, kv_bytes);
}
```

```cpp
#include <hip/hip_runtime.h>
#include <hip/hip_bf16.h>
#include <stdint.h>

namespace {

constexpr int D = 256, PAGE = 16, HKV = 8, G = 2, NSPLIT = 32, NW = 4;
constexpr int T = NW * NSPLIT;
constexpr unsigned PAGE_BYTES = PAGE * HKV * D * 2;
constexpr unsigned TOK_BYTES = HKV * D * 2;
constexpr unsigned HEAD_OF_GROUP = 0x54321076u;
constexpr unsigned CODE_SPAN = 5760;
constexpr float NEG_BIG = -3.0e38f;
constexpr int AUX_NT = 2, AUX_SC1 = 16;
constexpr unsigned WS_E = 0, WS_F = 4096, WS_P = WS_F + HKV * NSPLIT * 128, WS_BYTES = WS_P + HKV * NSPLIT * G * D * 4;
constexpr unsigned OOB = 0xF0000000u;
constexpr int REC = D + 4;
constexpr int LDS_TAG = NW * 2 * G * REC;
constexpr unsigned SPIN_CAP = 400000;

typedef unsigned u32x4 __attribute__((ext_vector_type(4)));
typedef unsigned u32x2 __attribute__((ext_vector_type(2)));
typedef float f32x4 __attribute__((ext_vector_type(4)));
typedef float f32x2 __attribute__((ext_vector_type(2)));
typedef __bf16 bf16x2 __attribute__((ext_vector_type(2)));
typedef const __attribute__((address_space(4))) int* cint_p;

__device__ __forceinline__ float dot2(unsigned a, unsigned b, float c) {
  return __builtin_amdgcn_fdot2_f32_bf16(__builtin_bit_cast(bf16x2, a), __builtin_bit_cast(bf16x2, b), c, false);
}
template <int CTRL> __device__ __forceinline__ float dpp(float x) {
  return __builtin_bit_cast(float, __builtin_amdgcn_mov_dpp(__builtin_bit_cast(int, x), CTRL, 0xf, 0xf, true));
}
constexpr int XOR1 = 0xB1, XOR2 = 0x4E, HMIRROR = 0x141, ROR8 = 0x128, ROR4 = 0x124, NEWBCAST = 0x150;

__device__ __forceinline__ float swap16_add(float a, float b) {
  auto s = __builtin_amdgcn_permlane16_swap(__float_as_uint(a), __float_as_uint(b), false, false);
  return __uint_as_float(s[0]) + __uint_as_float(s[1]);
}
__device__ __forceinline__ float xor16_max(float x) {
  auto s = __builtin_amdgcn_permlane16_swap(__float_as_uint(x), __float_as_uint(x), false, false);
  return fmaxf(__uint_as_float(s[0]), __uint_as_float(s[1]));
}
__device__ __forceinline__ float sum32(float x) {
  x += dpp<XOR1>(x); x += dpp<XOR2>(x); x += dpp<HMIRROR>(x); x += dpp<ROR8>(x);
  return swap16_add(x, x);
}
__device__ __forceinline__ float max32(float x) {
  x = fmaxf(x, dpp<XOR1>(x)); x = fmaxf(x, dpp<XOR2>(x)); x = fmaxf(x, dpp<HMIRROR>(x)); x = fmaxf(x, dpp<ROR8>(x));
  return xor16_max(x);
}

#define PIN() do { asm volatile("" ::: "memory"); __builtin_amdgcn_sched_barrier(0); } while (0)

#define TR_STEP(HALF, CTRL, BIT)                                            \
  _Pragma("unroll") for (int u = 0; u < (HALF); ++u) {                      \
    const float lo_ = x[u], hi_ = x[u + (HALF)];                            \
    const float t1_ = lo_ + dpp<CTRL>(lo_), t2_ = hi_ + dpp<CTRL>(hi_);     \
    x[u] = (lane & (BIT)) ? t2_ : t1_;                                      \
  }

struct Tile { u32x4 r[8]; };

__device__ __forceinline__ void issue(Tile& t, __amdgpu_buffer_rsrc_t rs, unsigned voff, unsigned soff) {
#pragma unroll
  for (int j = 0; j < 8; ++j) {
    t.r[j] = __builtin_amdgcn_raw_buffer_load_b128(rs, voff, soff + j * 2 * TOK_BYTES, AUX_NT);
    __builtin_amdgcn_sched_barrier(0);
  }
}

template <int J> __device__ __forceinline__ void pv_step(const u32x4& v, float p, float (&acc)[2][8]) {
  const float p0 = dpp<NEWBCAST + 2 * J>(p), p1 = dpp<NEWBCAST + 2 * J + 1>(p);
#pragma unroll
  for (int k = 0; k < 4; ++k) {
    const float lo = __uint_as_float(v[k] << 16), hi = __uint_as_float(v[k] & 0xffff0000u);
    acc[0][2 * k] = fmaf(p0, lo, acc[0][2 * k]);
    acc[0][2 * k + 1] = fmaf(p0, hi, acc[0][2 * k + 1]);
    acc[1][2 * k] = fmaf(p1, lo, acc[1][2 * k]);
    acc[1][2 * k + 1] = fmaf(p1, hi, acc[1][2 * k + 1]);
  }
  __builtin_amdgcn_sched_barrier(0);
}

__device__ __forceinline__ float rec_weights(const float* rec0, float (&wt)[2 * NW]) {
  f32x2 ml[2 * NW];
#pragma unroll
  for (int r = 0; r < 2 * NW; ++r) ml[r] = *reinterpret_cast<const f32x2*>(rec0 + r * G * REC);
  float M = NEG_BIG;
#pragma unroll
  for (int r = 0; r < 2 * NW; ++r) M = fmaxf(M, ml[r][0]);
  float L = 0.f;
#pragma unroll
  for (int r = 0; r < 2 * NW; ++r) {
    wt[r] = __builtin_amdgcn_exp2f(ml[r][0] - M);
    L = fmaf(wt[r], ml[r][1], L);
  }
  const float inv = L > 0.f ? __builtin_amdgcn_rcpf(L) : 0.f;
#pragma unroll
  for (int r = 0; r < 2 * NW; ++r) wt[r] *= inv;
  return L > 0.f ? M + __builtin_amdgcn_logf(L) : NEG_BIG;
}

__global__ void __launch_bounds__((NW + 1) * 64, 2)
attn_decode(const u32x4* __restrict__ q, const void* __restrict__ kc, const void* __restrict__ vc,
            const int* __restrict__ bt, const int* __restrict__ lens, unsigned char* __restrict__ ws,
            unsigned char* __restrict__ out, int nbps, unsigned kv_bytes) {
  __shared__ __attribute__((aligned(16))) float lds[LDS_TAG + 4];
  unsigned code_word;
  {
    const unsigned off = min((threadIdx.x & 63) * 128u, CODE_SPAN);
    asm volatile("s_getpc_b64 s[90:91]\n\tglobal_load_dword %0, %1, s[90:91]" : "=&v"(code_word) : "v"(off) : "s90", "s91");
    __builtin_amdgcn_sched_barrier(0);
  }
  asm volatile("" : "+s"(ws), "+s"(out) : "s"(q), "s"(kc), "s"(vc), "s"(bt), "s"(lens), "s"(nbps), "s"(kv_bytes));
  const int head = (HEAD_OF_GROUP >> (4 * (blockIdx.x & 7))) & 7, s = blockIdx.x >> 3;
  const int lane = threadIdx.x & 63;
  const int w = __builtin_amdgcn_readfirstlane(threadIdx.x >> 6);
  const int h = lane >> 5, c = lane & 31;
  const __amdgpu_buffer_rsrc_t wrs = __builtin_amdgcn_make_buffer_rsrc(ws, 0, WS_BYTES, 0x00020000);

  if (__builtin_expect(w < NW, 1)) {
    const __amdgpu_buffer_rsrc_t krs = __builtin_amdgcn_make_buffer_rsrc((void*)kc, 0, kv_bytes, 0x00020000);
    const __amdgpu_buffer_rsrc_t vrs = __builtin_amdgcn_make_buffer_rsrc((void*)vc, 0, kv_bytes, 0x00020000);
    const int wv = w * NSPLIT + s;
    const int bt0 = ((cint_p)bt)[min(wv, nbps - 1)];
    const int len_v = __builtin_nontemporal_load(lens);
    const int btv = bt[min(wv + lane * T, nbps - 1)];
    u32x4 qv[G];
#pragma unroll
    for (int g = 0; g < G; ++g) qv[g] = q[(head * G + g) * (D / 8) + c];
    __builtin_amdgcn_sched_barrier(0);
    const unsigned voff = h * TOK_BYTES + c * 16;
    const unsigned hoff = head * (D * 2);
    Tile K, V;
    {
      const unsigned o0 = (unsigned)bt0 * PAGE_BYTES + hoff;
      issue(K, krs, voff, o0);
      issue(V, vrs, voff, o0);
      PIN();
    }
    const int len_raw = __builtin_amdgcn_readfirstlane(len_v);
    const int skv = nbps * PAGE;
    const bool all_masked = len_raw <= 0;
    const int len = all_masked ? skv : min(len_raw, skv);
    const float sc = all_masked ? 0.f : 0.0625f * 1.4426950408889634f;
    const int npages = (len + PAGE - 1) / PAGE;
    const int n = npages > wv ? (npages - wv + T - 1) / T : 0;
    float m = NEG_BIG, l = 0.f, acc[2][8];
#pragma unroll
    for (int k = 0; k < 8; ++k) { acc[0][k] = 0.f; acc[1][k] = 0.f; }
    int tok = wv * PAGE + 2 * ((lane & 15) >> 1) + h;
    int wr_acc = (((w * 2 + h) * G) * REC + 4 + 8 * c) * 4;
    int wr_ml = ((w * 2 + h) * G + (c & 1)) * REC * 4;
    int rd_ml = (w & 1) * REC * 4, rd_acc = rd_ml + (4 + 128 * (w >> 1) + 2 * lane) * 4;
    int put_p = ((w & 1) * D + 128 * (w >> 1) + 2 * lane) * 4;
    asm volatile("" : "+v"(wr_acc), "+v"(wr_ml), "+v"(rd_ml), "+v"(rd_acc), "+v"(put_p));
    int i = 0;
    if (n > 0) do {
      float x[16];
#pragma unroll
      for (int j = 0; j < 8; ++j)
#pragma unroll
        for (int g = 0; g < G; ++g) {
          float d = 0.f;
#pragma unroll
          for (int k = 0; k < 4; ++k) d = dot2(K.r[j][k], qv[g][k], d);
          x[2 * j + g] = d;
        }
      ++i;
      const unsigned onx = (unsigned)__builtin_amdgcn_readlane(btv, i & 63) * PAGE_BYTES + hoff;
      const unsigned onext = i < n ? onx : OOB;
      PIN();
      issue(K, krs, voff, onext);
      PIN();
      TR_STEP(8, ROR8, 8) TR_STEP(4, HMIRROR, 4) TR_STEP(2, XOR2, 2) TR_STEP(1, XOR1, 1)
      float sv = swap16_add(x[0], x[0]) * sc;
      sv = tok < len ? sv : -INFINITY;
      tok += T * PAGE;
      float pm = fmaxf(sv, dpp<XOR2>(sv));
      pm = fmaxf(pm, dpp<ROR4>(pm));
      pm = fmaxf(pm, dpp<ROR8>(pm));
      const float mn = fmaxf(m, pm);
      if (__builtin_amdgcn_ballot_w64(mn > m)) {
        const float alpha = __builtin_amdgcn_exp2f(m - mn);
        l *= alpha;
        const float a0 = dpp<0x00>(alpha), a1 = dpp<0x55>(alpha);
#pragma unroll
        for (int k = 0; k < 8; ++k) { acc[0][k] *= a0; acc[1][k] *= a1; }
        m = mn;
      }
      const float p = __builtin_amdgcn_exp2f(sv - m);
      l += p;
      pv_step<0>(V.r[0], p, acc); pv_step<1>(V.r[1], p, acc);
      pv_step<2>(V.r[2], p, acc); pv_step<3>(V.r[3], p, acc);
      pv_step<4>(V.r[4], p, acc); pv_step<5>(V.r[5], p, acc);
      pv_step<6>(V.r[6], p, acc); pv_step<7>(V.r[7], p, acc);
      PIN();
      issue(V, vrs, voff, onext);
      PIN();
    } while (i < n);
    l += dpp<XOR2>(l);
    l += dpp<ROR4>(l);
    l += dpp<ROR8>(l);
    char* const lb = reinterpret_cast<char*>(lds);
#pragma unroll
    for (int g = 0; g < G; ++g) {
      *reinterpret_cast<f32x4*>(lb + wr_acc + g * REC * 4) = f32x4{acc[g][0], acc[g][1], acc[g][2], acc[g][3]};
      *reinterpret_cast<f32x4*>(lb + wr_acc + g * REC * 4 + 16) = f32x4{acc[g][4], acc[g][5], acc[g][6], acc[g][7]};
    }
    if (c < 2) *reinterpret_cast<f32x2*>(lb + wr_ml) = f32x2{m, l};
    __syncthreads();
    {
      const unsigned tag = __float_as_uint(lds[LDS_TAG]);
      f32x2 ra[2 * NW];
#pragma unroll
      for (int r = 0; r < 2 * NW; ++r) ra[r] = *reinterpret_cast<const f32x2*>(lb + rd_acc + r * G * REC * 4);
      float wt[2 * NW];
      rec_weights(reinterpret_cast<const float*>(lb + rd_ml), wt);
      f32x2 a = {0.f, 0.f};
#pragma unroll
      for (int r = 0; r < 2 * NW; ++r) a += wt[r] * ra[r];
      const u32x2 ta = {(__float_as_uint(a[0]) & ~0xffu) | (tag & 0xffu),
                        (__float_as_uint(a[1]) & ~0xffu) | ((tag >> 8) & 0xffu)};
      __builtin_amdgcn_raw_buffer_store_b64(ta, wrs, put_p, WS_P + (head * NSPLIT + s) * (G * D * 4), AUX_SC1);
    }
    PIN();
#pragma unroll
    for (int j = 0; j < 8; ++j) asm volatile("" ::"v"(K.r[j]), "v"(V.r[j]));
    asm volatile("" ::"v"(code_word));
    return;
  }

  const __amdgpu_buffer_rsrc_t ors = __builtin_amdgcn_make_buffer_rsrc(out, 0, HKV * G * D * 2, 0x00020000);
  const int g_out = s >> 4;
  const unsigned put_f = c == 0 ? WS_F + (head * NSPLIT + s) * 128 + h * 16 : OOB;
  const unsigned get_f = WS_F + (head * NSPLIT + c) * 128 + g_out * 16;
  const u32x2 e = __builtin_amdgcn_raw_buffer_load_b64(wrs, 0, WS_E + head * 256, AUX_SC1);
  const unsigned long long epoch = (((unsigned long long)e[1] << 32) | e[0]) + 1ull;
  const unsigned ep_lo = (unsigned)epoch, ep_hi = (unsigned)(epoch >> 32);
  const unsigned get = (c == s) ? OOB : WS_P + ((head * NSPLIT + c) * G + g_out) * (D * 4) + (16 * (s & 15) + 8 * h) * 4;
  lds[LDS_TAG] = __uint_as_float(ep_lo);
  asm volatile("" ::"v"(code_word));
  __syncthreads();
  u32x4 f = __builtin_amdgcn_raw_buffer_load_b128(wrs, get_f, 0, AUX_SC1);
  PIN();
  float wt[2 * NW];
  const unsigned ul = __float_as_uint(rec_weights(lds + h * REC, wt));
  __builtin_amdgcn_raw_buffer_store_b128(u32x4{ul, ep_lo, ep_lo, ep_hi}, wrs, put_f, 0, AUX_SC1);
  PIN();
  f32x4 pa = {0.f, 0.f, 0.f, 0.f}, pb = pa;
  {
    const float* rec = lds + g_out * REC + 4 + 16 * (s & 15) + 8 * h;
#pragma unroll
    for (int r = 0; r < 2 * NW; ++r) {
      const float wr = __uint_as_float(__builtin_amdgcn_readlane(__float_as_uint(wt[r]), g_out * 32));
      pa += wr * *reinterpret_cast<const f32x4*>(rec + r * G * REC);
      pb += wr * *reinterpret_cast<const f32x4*>(rec + r * G * REC + 4);
    }
  }
  if (c == s) f = u32x4{(unsigned)__builtin_amdgcn_readlane(ul, g_out * 32), ep_lo, ep_lo, ep_hi};
  auto not_ready = [&]() { return ((f[1] ^ ep_lo) | (f[2] ^ ep_lo) | (f[3] ^ ep_hi)) != 0; };
  bool wait_f = not_ready();
  if (__builtin_amdgcn_ballot_w64(wait_f) != 0) {
    unsigned spin = 0;
#pragma nounroll
    do {
      __builtin_amdgcn_s_sleep(2);
      asm volatile("" ::: "memory");
      if (wait_f) {
        f = __builtin_amdgcn_raw_buffer_load_b128(wrs, get_f, 0, AUX_SC1);
        wait_f = not_ready();
      }
    } while (__builtin_amdgcn_ballot_w64(wait_f) != 0 && ++spin < SPIN_CAP);
  }
  asm volatile("" ::: "memory");
  u32x4 ua = __builtin_amdgcn_raw_buffer_load_b128(wrs, get, 0, AUX_SC1);
  u32x4 ub = __builtin_amdgcn_raw_buffer_load_b128(wrs, get + 16, 0, AUX_SC1);
  __builtin_amdgcn_raw_buffer_store_b64(u32x2{ep_lo, ep_hi}, wrs, (lane == 0 && s == 0) ? WS_E + head * 256 : OOB, 0,
                                        AUX_SC1);
  PIN();
  const float lse = __uint_as_float(f[0]);
  const float M = max32(lse);
  float wgt = __builtin_amdgcn_exp2f(lse - M);
  wgt *= __builtin_amdgcn_rcpf(sum32(wgt));
  PIN();
  const unsigned t0 = ep_lo & 0xffu, t1 = (ep_lo >> 8) & 0xffu;
  auto stale = [&]() {
    const unsigned x0 = (ua[0] ^ t0) | (ua[2] ^ t0) | (ub[0] ^ t0) | (ub[2] ^ t0);
    const unsigned x1 = (ua[1] ^ t1) | (ua[3] ^ t1) | (ub[1] ^ t1) | (ub[3] ^ t1);
    return ((x0 | x1) & 0xffu) != 0 && c != s;
  };
  bool pending = stale();
  if (__builtin_amdgcn_ballot_w64(pending) != 0) {
    unsigned spin = 0;
#pragma nounroll
    do {
      asm volatile("" ::: "memory");
      if (pending) {
        ua = __builtin_amdgcn_raw_buffer_load_b128(wrs, get, 0, AUX_SC1);
        ub = __builtin_amdgcn_raw_buffer_load_b128(wrs, get + 16, 0, AUX_SC1);
        pending = stale();
      }
    } while (__builtin_amdgcn_ballot_w64(pending) != 0 && ++spin < SPIN_CAP);
  }
  float x[8];
#pragma unroll
  for (int k = 0; k < 4; ++k) {
    x[k] = wgt * (c == s ? pa[k] : __uint_as_float(ua[k] & ~0xffu));
    x[4 + k] = wgt * (c == s ? pb[k] : __uint_as_float(ub[k] & ~0xffu));
  }
#pragma unroll
  for (int u = 0; u < 4; ++u) x[u] = swap16_add(x[u], x[u + 4]);
  TR_STEP(2, ROR8, 8) TR_STEP(1, HMIRROR, 4)
  float o = x[0];
  o += dpp<XOR2>(o);
  o += dpp<XOR1>(o);
  const __bf16 ob = (__bf16)o;
  __builtin_amdgcn_raw_buffer_store_b16(__builtin_bit_cast(unsigned short, ob), ors,
                                        (lane & 3) == 0 ? (head * G * D + 16 * s + 8 * h + (c >> 2)) * 2 : OOB, 0, 0);
}

}

extern "C" void kernel_launch(void* const* d_in, const int* in_sizes, int n_in, void* d_out, int out_size,
                              void* d_ws, size_t ws_size, hipStream_t stream) {
  const int nbps = in_sizes[3];
  const unsigned kv_bytes = (unsigned)in_sizes[1] * 2u;
  attn_decode<<<HKV * NSPLIT, (NW + 1) * 64, 0, stream>>>((const u32x4*)d_in[0], d_in[1], d_in[2], (const int*)d_in[3],
                                                          (const int*)d_in[4], (unsigned char*)d_ws,
                                                          (unsigned char*)d_out, nbps, kv_bytes);
}
```
